```python
import torch

def _softmax(x, axis=-1):
    return torch.softmax(x, dim=axis)
def _gelu(x):
    return torch.nn.functional.gelu(x, approximate="tanh")
def _silu(x):
    return torch.nn.functional.silu(x)
def _sigmoid(x):
    return torch.sigmoid(x)
def _layernorm(x, axis=-1, eps=1e-5):
    m = x.mean(dim=axis, keepdim=True)
    v = x.var(dim=axis, keepdim=True, unbiased=False)
    return (x - m) / torch.sqrt(v + eps)
def _scatter_add(tgt, idx, src, axis=0):
    return tgt.clone().index_add_(axis, idx, src)
def _segment_sum(src, seg, n):
    out = torch.zeros((n,) + src.shape[1:], dtype=src.dtype)
    return out.index_add_(0, seg, src)
def _pad(x, pw):
    flat = tuple(v for pair in reversed(pw) for v in pair)
    return torch.nn.functional.pad(x, flat)

kernel_name = 'recurrent_scan__mha_block__moe_arch_mod__prompt'

def setup_inputs():

    _w = (lambda a: (lambda t: t.to(torch.bfloat16) if t.is_floating_point() else t)(torch.as_tensor(a)))
    def _A_setup():
        B, S, D, H = 1, 2048, 640, 32
        return {
            "x": _w((lambda *s: torch.randn(*s, dtype=torch.bfloat16) * 0.05)(B, S, D)),
            "wqkv": _w((lambda *s: torch.randn(*s, dtype=torch.bfloat16) * 0.05)(D, 3 * D)),
            "wo": _w((lambda *s: torch.randn(*s, dtype=torch.bfloat16) * 0.05)(D, D)),
        }
    _A_in = _A_setup()

    def _B_setup():
        B, S, D, F, C = 1, 2048, 640, 1536, 8
        return {
            "x": _w((lambda *s: torch.randn(*s, dtype=torch.bfloat16) * 0.05)(B, S, D)),
            "w_r": _w((lambda *s: torch.randn(*s, dtype=torch.bfloat16) * 0.05)(D)),
            "w_gate": _w((lambda *s: torch.randn(*s, dtype=torch.bfloat16) * 0.05)(D, F)),
            "w_up": _w((lambda *s: torch.randn(*s, dtype=torch.bfloat16) * 0.05)(D, F)),
            "w_down": _w((lambda *s: torch.randn(*s, dtype=torch.bfloat16) * 0.05)(F, D)),
        }
    _B_in = _B_setup()

    return {
        "A_x": _A_in["x"],
        "A_wqkv": _A_in["wqkv"],
        "A_wo": _A_in["wo"],
        "B_x": _B_in["x"],
        "B_w_r": _B_in["w_r"],
        "B_w_gate": _B_in["w_gate"],
        "B_w_up": _B_in["w_up"],
        "B_w_down": _B_in["w_down"],
    }

def reference(A_x, A_wqkv, A_wo, B_x, B_w_r, B_w_gate, B_w_up, B_w_down):
    def _A_ref(x, wqkv, wo):
        B, S, D = x.shape
        H = 32
        Dh = D // H
        qkv = (lambda x, *d: x.permute(*d))((x @ wqkv).reshape(B, S, 3, H, Dh), 2, 0, 3, 1, 4)
        q, k, v = qkv[0], qkv[1], qkv[2]
        scores = torch.einsum("bhqd,bhkd->bhqk", q, k) * (Dh ** -0.5)
        p = _softmax(scores, axis=-1)
        o = torch.einsum("bhqk,bhkd->bhqd", p, v)
        return (lambda x, *d: x.permute(*d))(o, 0, 2, 1, 3).reshape(B, S, D) @ wo

    def _B_ref(x, w_r, w_gate, w_up, w_down):
        B, S, D = x.shape
        C = 8
        r = x @ w_r
        idx = torch.argsort(-r, axis=-1)[:, :C]
        idx_e = (lambda x, n, axis: torch.repeat_interleave(x, n, dim=axis))(idx.reshape(B, C, 1), D, axis=2)
        x_sel = (lambda x, i, axis: torch.take_along_dim(x, i, dim=axis))(x, idx_e, axis=1)
        g = _silu(x_sel @ w_gate)
        u = x_sel @ w_up
        y = (g * u) @ w_down
        r_sel = _sigmoid((lambda x, i, axis: torch.take_along_dim(x, i, dim=axis))(r, idx, axis=1))
        y = y * r_sel.reshape(B, C, 1)
        b_off = (torch.arange(B) * S).reshape(B, 1)
        flat_idx = (idx + b_off).reshape(B * C)
        zero = (x * 0.0).reshape(B * S, D)
        upd = _scatter_add(zero, flat_idx, y.reshape(B * C, D), axis=0)
        return x + upd.reshape(B, S, D)

    x = A_x
    xn = _layernorm(x, axis=-1)
    s = _A_ref(xn, A_wqkv, A_wo)
    g = _silu(_B_ref(xn, B_w_r, B_w_gate, B_w_up, B_w_down))
    return x + s * g
```

```cpp
#include <hip/hip_runtime.h>
#include <stdint.h>

#define DI __device__ __forceinline__
#define LAS __attribute__((address_space(3)))
#define GAS __attribute__((address_space(1)))
typedef short bf16x8 __attribute__((ext_vector_type(8)));
typedef short bf16x4 __attribute__((ext_vector_type(4)));
typedef __bf16 bf16x2 __attribute__((ext_vector_type(2)));
typedef float f32x2 __attribute__((ext_vector_type(2)));
typedef float f32x4 __attribute__((ext_vector_type(4)));
typedef float f32x8 __attribute__((ext_vector_type(8)));
typedef float f32x16 __attribute__((ext_vector_type(16)));
typedef unsigned u32x2 __attribute__((ext_vector_type(2)));
typedef unsigned u32x4 __attribute__((ext_vector_type(4)));
typedef unsigned long long u64;

constexpr unsigned WS_NONCE = 0, WS_SEL = 20480, FS = 256;
constexpr unsigned WS_ST = 32768, WS_R = 49152, WS_H = 65536, WS_Y = 90112, WS_XN = 102400;
constexpr unsigned PT = 5120, WS_KV = 131072, WS_Q = WS_KV + 16 * 64 * PT, WS_O = WS_Q + 2048 * 1280, WS_GF = WS_O + 2048 * 1280, WS_OF = WS_GF + 256 * FS, WS_HF = WS_OF + 256 * FS, WS_YF = WS_HF + 192 * FS, WS_NF = WS_YF + 64 * FS, WS_END = WS_NF + 16 * FS;
constexpr unsigned L_WOA = 0, L_WOB = 81920;
constexpr unsigned L_WD = 102400, L_CMB = 102400, L_FX = 102400, L_GATE = 131072, L_UP = 141376;
constexpr unsigned L_MU = 155648, L_RS = 156160, L_CS = 156672;
constexpr unsigned L_PUB = 158720, L_WOD = 158724, L_READY = 158788, L_FIN = 158728, L_AUX = 158732, L_BAD = 158736, L_WOK = 158740, L_OMASK = 158744, L_MAXC = 158748, L_PAIR = 158752, L_DEC = 158784, L_SEL = 158800, L_END = 158832;
constexpr unsigned ONE2 = 0x3F803F80u;
constexpr unsigned SPIN_CAP = 1u << 18;
constexpr int SC1 = 16;

#define RLX_AGENT __ATOMIC_RELAXED, __HIP_MEMORY_SCOPE_AGENT
#define RLX_WG __ATOMIC_RELAXED, __HIP_MEMORY_SCOPE_WORKGROUP
#define BAR() asm volatile("s_waitcnt lgkmcnt(0)\n\ts_barrier" ::: "memory")
#define BARV(n) asm volatile("s_waitcnt vmcnt(" #n ") lgkmcnt(0)\n\ts_barrier" ::: "memory")
#define PACE(n) asm volatile("s_waitcnt vmcnt(%c0)" :: "i"(n) : "memory")
#define DRAIN() asm volatile("s_waitcnt vmcnt(0)" ::: "memory")
#define MFMA16(a, b, c) __builtin_amdgcn_mfma_f32_16x16x32_bf16(a, b, c, 0, 0, 0)
#define MFMA32(a, b, c) __builtin_amdgcn_mfma_f32_32x32x16_bf16(a, b, c, 0, 0, 0)
#define MFMA32K8(a, b, c) __builtin_amdgcn_mfma_f32_32x32x8bf16_1k(a, b, c, 0, 0, 0)

template <typename T> DI T lds_ld(unsigned a) { return *(const LAS T*)(uintptr_t)a; }
template <typename T> DI void lds_st(unsigned a, T v) { *(LAS T*)(uintptr_t)a = v; }
DI bf16x8 lds_tr8(unsigned a0, unsigned a1) {
  const bf16x4 lo = __builtin_amdgcn_ds_read_tr16_b64_v4i16((LAS bf16x4*)(uintptr_t)a0), hi = __builtin_amdgcn_ds_read_tr16_b64_v4i16((LAS bf16x4*)(uintptr_t)a1);
  return __builtin_shufflevector(lo, hi, 0, 1, 2, 3, 4, 5, 6, 7);
}
DI unsigned pk2(float lo, float hi) { const bf16x2 p = {(__bf16)lo, (__bf16)hi}; return __builtin_bit_cast(unsigned, p); }
DI float bfr(float x) { return (float)(__bf16)x; }
DI float blo(unsigned u) { return __uint_as_float(u << 16); }
DI float bhi(unsigned u) { return __uint_as_float(u & 0xffff0000u); }
DI float xsum(float x) {
  const auto s = __builtin_amdgcn_permlane16_swap(__float_as_uint(x), __float_as_uint(x), false, false);
  x = __uint_as_float(s[0]) + __uint_as_float(s[1]);
  const auto t = __builtin_amdgcn_permlane32_swap(__float_as_uint(x), __float_as_uint(x), false, false);
  return __uint_as_float(t[0]) + __uint_as_float(t[1]);
}
DI unsigned ln2(unsigned x2, float mu, float rs) { const unsigned c = pk2(blo(x2) - mu, bhi(x2) - mu); return pk2(blo(c) * rs, bhi(c) * rs); }
DI float sigm(float x) { return __builtin_amdgcn_rcpf(1.0f + __builtin_amdgcn_exp2f(-1.4426950408889634f * x)); }
DI int fresh_lane() { int l; asm volatile("v_mbcnt_lo_u32_b32 %0, -1, 0\n\tv_mbcnt_hi_u32_b32 %0, -1, %0" : "=v"(l)); return l; }
DI unsigned uni(unsigned x) { return __builtin_amdgcn_readfirstlane(x); }
DI u64 make_tag(u64 n) { asm volatile("" : "+v"(n)); return (((u64)uni((unsigned)(n >> 32)) << 32) | uni((unsigned)n)) + 1; }
DI void dma(const char* base, unsigned voff, unsigned dst) {
  unsigned keep;
  asm volatile("s_mov_b32 %0, m0\n\ts_mov_b32 m0, %3\n\ts_nop 0\n\tglobal_load_lds_dwordx4 %1, %2\n\ts_mov_b32 m0, %0" : "=&s"(keep) : "v"(voff), "s"(base), "s"(dst) : "memory");
}
DI void dma_masked(const char* base, unsigned voff, unsigned dst, u64 mask) {
  unsigned keep;
  asm volatile("s_mov_b32 %0, m0\n\ts_mov_b32 m0, %3\n\ts_mov_b64 exec, %4\n\tglobal_load_lds_dwordx4 %1, %2\n\ts_mov_b64 exec, -1\n\ts_mov_b32 m0, %0"
               : "=&s"(keep) : "v"(voff), "s"(base), "s"(dst), "s"(mask) : "memory");
}
template <int SLEEP, int N> DI void wait_flags(const char* ws, const unsigned (&off)[N], u64 tag) {
  u64 v[N];
#pragma unroll
  for (int i = 0; i < N; ++i) v[i] = ~tag;
#pragma unroll 1
  for (unsigned spin = 0; spin < SPIN_CAP; ++spin) {
    bool ok = true;
#pragma unroll
    for (int i = 0; i < N; ++i) if (v[i] != tag) v[i] = __hip_atomic_load((GAS u64*)(ws + off[i]), RLX_AGENT);
#pragma unroll
    for (int i = 0; i < N; ++i) ok &= v[i] == tag;
    if (__all(ok)) break;
    __builtin_amdgcn_s_sleep(SLEEP);
  }
  __builtin_amdgcn_fence(__ATOMIC_ACQUIRE, "wavefront");
}
template <int SLEEP = 0> DI unsigned wait_granule(const char* ws, unsigned off, unsigned tag) {
  u64 v;
#pragma unroll 1
  for (unsigned spin = 0; spin < SPIN_CAP; ++spin) {
    v = __hip_atomic_load((GAS u64*)(ws + off), RLX_AGENT);
    if (__all((unsigned)(v >> 32) == tag)) break;
    if (SLEEP) __builtin_amdgcn_s_sleep(SLEEP);
  }
  __builtin_amdgcn_fence(__ATOMIC_ACQUIRE, "wavefront");
  return (unsigned)v;
}
template <int SLEEP = 1> DI void wait_lds(unsigned a, unsigned want) {
#pragma unroll 1
  for (unsigned spin = 0; spin < SPIN_CAP; ++spin) {
    if (uni(__hip_atomic_load((LAS unsigned*)(uintptr_t)a, RLX_WG)) >= want) break;
    if (SLEEP) __builtin_amdgcn_s_sleep(SLEEP);
  }
  asm volatile("" ::: "memory");
}
DI u32x2 neg_split(float m) { const float n = -m, a = bfr(n), b = bfr(n - a), c = (n - a) - b; return u32x2{pk2(a, b), pk2(c, 0.f)}; }
DI float max16(const f32x16& s) {
  float a = fmaxf(fmaxf(s[0], s[1]), s[2]), b = fmaxf(fmaxf(s[3], s[4]), s[5]);
  a = fmaxf(fmaxf(a, s[6]), s[7]); b = fmaxf(fmaxf(b, s[8]), s[9]); a = fmaxf(fmaxf(a, s[10]), s[11]); b = fmaxf(fmaxf(b, s[12]), s[13]);
  return fmaxf(fmaxf(a, s[14]), fmaxf(b, s[15]));
}
DI float xmax32(float x) { const auto t = __builtin_amdgcn_permlane32_swap(__float_as_uint(x), __float_as_uint(x), false, false); return fmaxf(__uint_as_float(t[0]), __uint_as_float(t[1])); }

DI void dma8(const char* base, const unsigned* v, unsigned dst) {
  unsigned keep;
  asm volatile("s_mov_b32 %0, m0\n\ts_mov_b32 m0, %10\n\ts_nop 0\n\t"
               "global_load_lds_dwordx4 %1, %9 offset:-4096\n\tglobal_load_lds_dwordx4 %2, %9 offset:-3072\n\tglobal_load_lds_dwordx4 %3, %9 offset:-2048\n\t"
               "global_load_lds_dwordx4 %4, %9 offset:-1024\n\tglobal_load_lds_dwordx4 %5, %9\n\tglobal_load_lds_dwordx4 %6, %9 offset:1024\n\t"
               "global_load_lds_dwordx4 %7, %9 offset:2048\n\tglobal_load_lds_dwordx4 %8, %9 offset:3072\n\ts_mov_b32 m0, %0"
               : "=&s"(keep) : "v"(v[0]), "v"(v[1]), "v"(v[2]), "v"(v[3]), "v"(v[4]), "v"(v[5]), "v"(v[6]), "v"(v[7]), "s"(base), "s"(dst + 4096) : "memory");
}

template <int ORI> DI void g_phase(char* ws, int lane, int w, int ct, int grb, __amdgpu_buffer_rsrc_t rs_ws, u64 nonce) {
  const int rh = w & 1, TP = w >> 1, l15 = lane & 15, g = lane >> 4, q = l15 >> 2, p = lane & 3;
  f32x16 acc[4]; f32x8 cs; u32x4 dst;
#pragma unroll
  for (int j = 0; j < 4; ++j) {
    const int n = 4 * TP + (j ^ (2 * rh));
    if (ORI == 0) {
      const int sec = ct >= 5, pp = 16 * (ct - 5 * sec) + 2 * n + (g >> 1), hq = pp / 5, i = pp - 5 * hq, tok = 16 * (g & 1) + l15;
      dst[j] = (sec ? WS_KV + (hq * 64 + 4 * grb + 2 * rh) * PT : WS_Q + (hq * 64 + 4 * grb + 2 * rh) * 2560) + (i == 2 ? 2048 : (i > 2 ? 1024 - 3 * 512 : 0) + i * 512) + tok * 16;
    } else {
      const int c = 128 * (ct - 10) + 16 * n + l15, hd = c / 20, d = c - 20 * hd;
      dst[j] = WS_KV + ((hd >> 1) * 64 + 4 * grb + 2 * rh) * PT + 2560 + (hd & 1) * 1280 + (g >> 1) * 640 + (g & 1) * 320 + d * 16;
    }
  }
  unsigned sa = L_MU + (64 * rh + (ORI == 0 ? l15 : 4 * g)) * 4, ca = L_CS + (64 * TP + (ORI == 0 ? 4 * g : l15)) * 4;
  {
    const unsigned sw = l15 >> 1, xrow = (64 * rh + l15) * 128;
    unsigned xa0 = xrow + ((g ^ sw) << 4), xa1 = xrow + (((4 + g) ^ sw) << 4);
    const unsigned X = ((q >> 1) | ((g & 1) << 1)) ^ (2 * rh), wrow = 16384 + TP * 8192 + (16 * (g >> 1) + 4 * (g & 1) + q) * 128 + 8 * p;
    u32x4 wa = {wrow + (X << 5), wrow + ((1 ^ X) << 5), wrow + ((2 ^ X) << 5), wrow + ((3 ^ X) << 5)};
#define G_OPERANDS : "={v[0:15]}"(acc[0]), "={v[16:31]}"(acc[1]), "={v[32:47]}"(acc[2]), "={v[48:63]}"(acc[3]), "={v[136:143]}"(cs), \
                     "+{v128}"(xa0), "+{v129}"(xa1), "+{v[130:133]}"(wa), "+v"(dst), "+v"(sa), "+v"(ca) : "{s45}"(32768u) : "v64","v65","v66","v67","v68","v69","v70","v71","v72","v73","v74","v75","v76","v77","v78","v79","v80","v81","v82","v83","v84","v85","v86","v87","v88","v89","v90","v91","v92","v93","v94","v95","v96","v97","v98","v99","v100","v101","v102","v103","v104","v105","v106","v107","v108","v109","v110","v111","v112","v113","v114","v115","v116","v117","v118","v119","v120","v121","v122","v123","v124","v125","v126","v127","v144","v145","v146","v147", "s40", "s41", "s42", "scc", "memory"
    if (ORI == 0) asm volatile(
      "v_mov_b32 v0, 0\n"
      "v_mov_b32 v1, 0\n"
      "v_mov_b32 v2, 0\n"
      "v_mov_b32 v3, 0\n"
      "v_mov_b32 v4, 0\n"
      "v_mov_b32 v5, 0\n"
      "v_mov_b32 v6, 0\n"
      "v_mov_b32 v7, 0\n"
      "v_mov_b32 v8, 0\n"
      "v_mov_b32 v9, 0\n"
      "v_mov_b32 v10, 0\n"
      "v_mov_b32 v11, 0\n"
      "v_mov_b32 v12, 0\n"
      "v_mov_b32 v13, 0\n"
      "v_mov_b32 v14, 0\n"
      "v_mov_b32 v15, 0\n"
      "v_mov_b32 v16, 0\n"
      "v_mov_b32 v17, 0\n"
      "v_mov_b32 v18, 0\n"
      "v_mov_b32 v19, 0\n"
      "v_mov_b32 v20, 0\n"
      "v_mov_b32 v21, 0\n"
      "v_mov_b32 v22, 0\n"
      "v_mov_b32 v23, 0\n"
      "v_mov_b32 v24, 0\n"
      "v_mov_b32 v25, 0\n"
      "v_mov_b32 v26, 0\n"
      "v_mov_b32 v27, 0\n"
      "v_mov_b32 v28, 0\n"
      "v_mov_b32 v29, 0\n"
      "v_mov_b32 v30, 0\n"
      "v_mov_b32 v31, 0\n"
      "v_mov_b32 v32, 0\n"
      "v_mov_b32 v33, 0\n"
      "v_mov_b32 v34, 0\n"
      "v_mov_b32 v35, 0\n"
      "v_mov_b32 v36, 0\n"
      "v_mov_b32 v37, 0\n"
      "v_mov_b32 v38, 0\n"
      "v_mov_b32 v39, 0\n"
      "v_mov_b32 v40, 0\n"
      "v_mov_b32 v41, 0\n"
      "v_mov_b32 v42, 0\n"
      "v_mov_b32 v43, 0\n"
      "v_mov_b32 v44, 0\n"
      "v_mov_b32 v45, 0\n"
      "v_mov_b32 v46, 0\n"
      "v_mov_b32 v47, 0\n"
      "v_mov_b32 v48, 0\n"
      "v_mov_b32 v49, 0\n"
      "v_mov_b32 v50, 0\n"
      "v_mov_b32 v51, 0\n"
      "v_mov_b32 v52, 0\n"
      "v_mov_b32 v53, 0\n"
      "v_mov_b32 v54, 0\n"
      "v_mov_b32 v55, 0\n"
      "v_mov_b32 v56, 0\n"
      "v_mov_b32 v57, 0\n"
      "v_mov_b32 v58, 0\n"
      "v_mov_b32 v59, 0\n"
      "v_mov_b32 v60, 0\n"
      "v_mov_b32 v61, 0\n"
      "v_mov_b32 v62, 0\n"
      "v_mov_b32 v63, 0\n"
      "v_mov_b32 v136, 0\n"
      "v_mov_b32 v137, 0\n"
      "v_mov_b32 v138, 0\n"
      "v_mov_b32 v139, 0\n"
      "v_mov_b32 v140, 0\n"
      "v_mov_b32 v141, 0\n"
      "v_mov_b32 v142, 0\n"
      "v_mov_b32 v143, 0\n"
      "v_mov_b32 v144, 0x3f803f80\n"
      "v_mov_b32 v145, 0x3f803f80\n"
      "v_mov_b32 v146, 0x3f803f80\n"
      "v_mov_b32 v147, 0x3f803f80\n"
      "s_mov_b32 s40, 0\n"
      "s_mov_b32 s41, s45\n"
      "s_waitcnt lgkmcnt(0)\n"
      "s_barrier\n"
      "ds_read_b128 v[64:67], v128 offset:0\n"
      "ds_read_b64_tr_b16 v[80:81], v130 offset:0\n"
      "ds_read_b64_tr_b16 v[82:83], v130 offset:1024\n"
      "ds_read_b128 v[68:71], v128 offset:2048\n"
      "ds_read_b128 v[72:75], v128 offset:4096\n"
      "ds_read_b128 v[76:79], v128 offset:6144\n"
      "ds_read_b64_tr_b16 v[84:85], v131 offset:0\n"
      "ds_read_b64_tr_b16 v[86:87], v131 offset:1024\n"
      "ds_read_b64_tr_b16 v[88:89], v132 offset:0\n"
      "ds_read_b64_tr_b16 v[90:91], v132 offset:1024\n"
      "ds_read_b64_tr_b16 v[92:93], v133 offset:0\n"
      "ds_read_b64_tr_b16 v[94:95], v133 offset:1024\n"
      "s_waitcnt lgkmcnt(0)\n"
      "Lg_%=:\n"
      "v_mfma_f32_16x16x32_bf16 v[0:3], v[80:83], v[64:67], v[0:3]\n"
      "ds_read_b128 v[96:99], v129 offset:0\n"
      "v_mfma_f32_16x16x32_bf16 v[4:7], v[80:83], v[68:71], v[4:7]\n"
      "ds_read_b64_tr_b16 v[112:113], v130 offset:4096\n"
      "v_mfma_f32_16x16x32_bf16 v[8:11], v[80:83], v[72:75], v[8:11]\n"
      "ds_read_b64_tr_b16 v[114:115], v130 offset:5120\n"
      "v_mfma_f32_16x16x32_bf16 v[12:15], v[80:83], v[76:79], v[12:15]\n"
      "ds_read_b128 v[100:103], v129 offset:2048\n"
      "v_mfma_f32_16x16x32_bf16 v[136:139], v[80:83], v[144:147], v[136:139]\n"
      "ds_read_b128 v[104:107], v129 offset:4096\n"
      "v_mfma_f32_16x16x32_bf16 v[16:19], v[84:87], v[64:67], v[16:19]\n"
      "ds_read_b128 v[108:111], v129 offset:6144\n"
      "v_mfma_f32_16x16x32_bf16 v[20:23], v[84:87], v[68:71], v[20:23]\n"
      "ds_read_b64_tr_b16 v[116:117], v131 offset:4096\n"
      "v_mfma_f32_16x16x32_bf16 v[24:27], v[84:87], v[72:75], v[24:27]\n"
      "ds_read_b64_tr_b16 v[118:119], v131 offset:5120\n"
      "v_mfma_f32_16x16x32_bf16 v[28:31], v[84:87], v[76:79], v[28:31]\n"
      "ds_read_b64_tr_b16 v[120:121], v132 offset:4096\n"
      "v_mfma_f32_16x16x32_bf16 v[140:143], v[84:87], v[144:147], v[140:143]\n"
      "ds_read_b64_tr_b16 v[122:123], v132 offset:5120\n"
      "v_mfma_f32_16x16x32_bf16 v[32:35], v[88:91], v[64:67], v[32:35]\n"
      "ds_read_b64_tr_b16 v[124:125], v133 offset:4096\n"
      "v_mfma_f32_16x16x32_bf16 v[36:39], v[88:91], v[68:71], v[36:39]\n"
      "ds_read_b64_tr_b16 v[126:127], v133 offset:5120\n"
      "v_mfma_f32_16x16x32_bf16 v[40:43], v[88:91], v[72:75], v[40:43]\n"
      "v_add_u32 v128, s41, v128\n"
      "v_add_u32 v130, s41, v130\n"
      "v_mfma_f32_16x16x32_bf16 v[44:47], v[88:91], v[76:79], v[44:47]\n"
      "v_add_u32 v131, s41, v131\n"
      "v_mfma_f32_16x16x32_bf16 v[48:51], v[92:95], v[64:67], v[48:51]\n"
      "v_add_u32 v132, s41, v132\n"
      "v_mfma_f32_16x16x32_bf16 v[52:55], v[92:95], v[68:71], v[52:55]\n"
      "v_add_u32 v133, s41, v133\n"
      "v_mfma_f32_16x16x32_bf16 v[56:59], v[92:95], v[72:75], v[56:59]\n"
      "v_add_u32 v129, s41, v129\n"
      "v_mfma_f32_16x16x32_bf16 v[60:63], v[92:95], v[76:79], v[60:63]\n"
      "s_waitcnt lgkmcnt(0)\n"
      "v_mfma_f32_16x16x32_bf16 v[0:3], v[112:115], v[96:99], v[0:3]\n"
      "v_mfma_f32_16x16x32_bf16 v[4:7], v[112:115], v[100:103], v[4:7]\n"
      "s_barrier\n"
      "v_mfma_f32_16x16x32_bf16 v[8:11], v[112:115], v[104:107], v[8:11]\n"
      "ds_read_b128 v[64:67], v128 offset:0\n"
      "v_mfma_f32_16x16x32_bf16 v[12:15], v[112:115], v[108:111], v[12:15]\n"
      "ds_read_b64_tr_b16 v[80:81], v130 offset:0\n"
      "v_mfma_f32_16x16x32_bf16 v[136:139], v[112:115], v[144:147], v[136:139]\n"
      "ds_read_b64_tr_b16 v[82:83], v130 offset:1024\n"
      "v_mfma_f32_16x16x32_bf16 v[16:19], v[116:119], v[96:99], v[16:19]\n"
      "ds_read_b128 v[68:71], v128 offset:2048\n"
      "v_mfma_f32_16x16x32_bf16 v[20:23], v[116:119], v[100:103], v[20:23]\n"
      "ds_read_b128 v[72:75], v128 offset:4096\n"
      "v_mfma_f32_16x16x32_bf16 v[24:27], v[116:119], v[104:107], v[24:27]\n"
      "ds_read_b128 v[76:79], v128 offset:6144\n"
      "v_mfma_f32_16x16x32_bf16 v[28:31], v[116:119], v[108:111], v[28:31]\n"
      "ds_read_b64_tr_b16 v[84:85], v131 offset:0\n"
      "v_mfma_f32_16x16x32_bf16 v[140:143], v[116:119], v[144:147], v[140:143]\n"
      "ds_read_b64_tr_b16 v[86:87], v131 offset:1024\n"
      "v_mfma_f32_16x16x32_bf16 v[32:35], v[120:123], v[96:99], v[32:35]\n"
      "ds_read_b64_tr_b16 v[88:89], v132 offset:0\n"
      "v_mfma_f32_16x16x32_bf16 v[36:39], v[120:123], v[100:103], v[36:39]\n"
      "ds_read_b64_tr_b16 v[90:91], v132 offset:1024\n"
      "v_mfma_f32_16x16x32_bf16 v[40:43], v[120:123], v[104:107], v[40:43]\n"
      "ds_read_b64_tr_b16 v[92:93], v133 offset:0\n"
      "v_mfma_f32_16x16x32_bf16 v[44:47], v[120:123], v[108:111], v[44:47]\n"
      "ds_read_b64_tr_b16 v[94:95], v133 offset:1024\n"
      "v_mfma_f32_16x16x32_bf16 v[48:51], v[124:127], v[96:99], v[48:51]\n"
      "s_add_u32 s40, s40, 1\n"
      "s_and_b32 s42, s40, 3\n"
      "v_mfma_f32_16x16x32_bf16 v[52:55], v[124:127], v[100:103], v[52:55]\n"
      "s_cmp_eq_u32 s42, 3\n"
      "s_cselect_b32 s41, 0xfffe8000, s45\n"
      "v_mfma_f32_16x16x32_bf16 v[56:59], v[124:127], v[104:107], v[56:59]\n"
      "s_cmp_lg_u32 s40, 10\n"
      "v_mfma_f32_16x16x32_bf16 v[60:63], v[124:127], v[108:111], v[60:63]\n"
      "s_waitcnt lgkmcnt(0)\n"
      "s_cbranch_scc1 Lg_%=\n"
      "s_nop 7\n"
      G_OPERANDS);
    else asm volatile(
      "v_mov_b32 v0, 0\n"
      "v_mov_b32 v1, 0\n"
      "v_mov_b32 v2, 0\n"
      "v_mov_b32 v3, 0\n"
      "v_mov_b32 v4, 0\n"
      "v_mov_b32 v5, 0\n"
      "v_mov_b32 v6, 0\n"
      "v_mov_b32 v7, 0\n"
      "v_mov_b32 v8, 0\n"
      "v_mov_b32 v9, 0\n"
      "v_mov_b32 v10, 0\n"
      "v_mov_b32 v11, 0\n"
      "v_mov_b32 v12, 0\n"
      "v_mov_b32 v13, 0\n"
      "v_mov_b32 v14, 0\n"
      "v_mov_b32 v15, 0\n"
      "v_mov_b32 v16, 0\n"
      "v_mov_b32 v17, 0\n"
      "v_mov_b32 v18, 0\n"
      "v_mov_b32 v19, 0\n"
      "v_mov_b32 v20, 0\n"
      "v_mov_b32 v21, 0\n"
      "v_mov_b32 v22, 0\n"
      "v_mov_b32 v23, 0\n"
      "v_mov_b32 v24, 0\n"
      "v_mov_b32 v25, 0\n"
      "v_mov_b32 v26, 0\n"
      "v_mov_b32 v27, 0\n"
      "v_mov_b32 v28, 0\n"
      "v_mov_b32 v29, 0\n"
      "v_mov_b32 v30, 0\n"
      "v_mov_b32 v31, 0\n"
      "v_mov_b32 v32, 0\n"
      "v_mov_b32 v33, 0\n"
      "v_mov_b32 v34, 0\n"
      "v_mov_b32 v35, 0\n"
      "v_mov_b32 v36, 0\n"
      "v_mov_b32 v37, 0\n"
      "v_mov_b32 v38, 0\n"
      "v_mov_b32 v39, 0\n"
      "v_mov_b32 v40, 0\n"
      "v_mov_b32 v41, 0\n"
      "v_mov_b32 v42, 0\n"
      "v_mov_b32 v43, 0\n"
      "v_mov_b32 v44, 0\n"
      "v_mov_b32 v45, 0\n"
      "v_mov_b32 v46, 0\n"
      "v_mov_b32 v47, 0\n"
      "v_mov_b32 v48, 0\n"
      "v_mov_b32 v49, 0\n"
      "v_mov_b32 v50, 0\n"
      "v_mov_b32 v51, 0\n"
      "v_mov_b32 v52, 0\n"
      "v_mov_b32 v53, 0\n"
      "v_mov_b32 v54, 0\n"
      "v_mov_b32 v55, 0\n"
      "v_mov_b32 v56, 0\n"
      "v_mov_b32 v57, 0\n"
      "v_mov_b32 v58, 0\n"
      "v_mov_b32 v59, 0\n"
      "v_mov_b32 v60, 0\n"
      "v_mov_b32 v61, 0\n"
      "v_mov_b32 v62, 0\n"
      "v_mov_b32 v63, 0\n"
      "v_mov_b32 v136, 0\n"
      "v_mov_b32 v137, 0\n"
      "v_mov_b32 v138, 0\n"
      "v_mov_b32 v139, 0\n"
      "v_mov_b32 v140, 0\n"
      "v_mov_b32 v141, 0\n"
      "v_mov_b32 v142, 0\n"
      "v_mov_b32 v143, 0\n"
      "v_mov_b32 v144, 0x3f803f80\n"
      "v_mov_b32 v145, 0x3f803f80\n"
      "v_mov_b32 v146, 0x3f803f80\n"
      "v_mov_b32 v147, 0x3f803f80\n"
      "s_mov_b32 s40, 0\n"
      "s_mov_b32 s41, s45\n"
      "s_waitcnt lgkmcnt(0)\n"
      "s_barrier\n"
      "ds_read_b128 v[64:67], v128 offset:0\n"
      "ds_read_b64_tr_b16 v[80:81], v130 offset:0\n"
      "ds_read_b64_tr_b16 v[82:83], v130 offset:1024\n"
      "ds_read_b128 v[68:71], v128 offset:2048\n"
      "ds_read_b128 v[72:75], v128 offset:4096\n"
      "ds_read_b128 v[76:79], v128 offset:6144\n"
      "ds_read_b64_tr_b16 v[84:85], v131 offset:0\n"
      "ds_read_b64_tr_b16 v[86:87], v131 offset:1024\n"
      "ds_read_b64_tr_b16 v[88:89], v132 offset:0\n"
      "ds_read_b64_tr_b16 v[90:91], v132 offset:1024\n"
      "ds_read_b64_tr_b16 v[92:93], v133 offset:0\n"
      "ds_read_b64_tr_b16 v[94:95], v133 offset:1024\n"
      "s_waitcnt lgkmcnt(0)\n"
      "Lg_%=:\n"
      "v_mfma_f32_16x16x32_bf16 v[0:3], v[64:67], v[80:83], v[0:3]\n"
      "ds_read_b128 v[96:99], v129 offset:0\n"
      "v_mfma_f32_16x16x32_bf16 v[4:7], v[68:71], v[80:83], v[4:7]\n"
      "ds_read_b64_tr_b16 v[112:113], v130 offset:4096\n"
      "v_mfma_f32_16x16x32_bf16 v[8:11], v[72:75], v[80:83], v[8:11]\n"
      "ds_read_b64_tr_b16 v[114:115], v130 offset:5120\n"
      "v_mfma_f32_16x16x32_bf16 v[12:15], v[76:79], v[80:83], v[12:15]\n"
      "ds_read_b128 v[100:103], v129 offset:2048\n"
      "v_mfma_f32_16x16x32_bf16 v[136:139], v[144:147], v[80:83], v[136:139]\n"
      "ds_read_b128 v[104:107], v129 offset:4096\n"
      "v_mfma_f32_16x16x32_bf16 v[16:19], v[64:67], v[84:87], v[16:19]\n"
      "ds_read_b128 v[108:111], v129 offset:6144\n"
      "v_mfma_f32_16x16x32_bf16 v[20:23], v[68:71], v[84:87], v[20:23]\n"
      "ds_read_b64_tr_b16 v[116:117], v131 offset:4096\n"
      "v_mfma_f32_16x16x32_bf16 v[24:27], v[72:75], v[84:87], v[24:27]\n"
      "ds_read_b64_tr_b16 v[118:119], v131 offset:5120\n"
      "v_mfma_f32_16x16x32_bf16 v[28:31], v[76:79], v[84:87], v[28:31]\n"
      "ds_read_b64_tr_b16 v[120:121], v132 offset:4096\n"
      "v_mfma_f32_16x16x32_bf16 v[140:143], v[144:147], v[84:87], v[140:143]\n"
      "ds_read_b64_tr_b16 v[122:123], v132 offset:5120\n"
      "v_mfma_f32_16x16x32_bf16 v[32:35], v[64:67], v[88:91], v[32:35]\n"
      "ds_read_b64_tr_b16 v[124:125], v133 offset:4096\n"
      "v_mfma_f32_16x16x32_bf16 v[36:39], v[68:71], v[88:91], v[36:39]\n"
      "ds_read_b64_tr_b16 v[126:127], v133 offset:5120\n"
      "v_mfma_f32_16x16x32_bf16 v[40:43], v[72:75], v[88:91], v[40:43]\n"
      "v_add_u32 v128, s41, v128\n"
      "v_add_u32 v130, s41, v130\n"
      "v_mfma_f32_16x16x32_bf16 v[44:47], v[76:79], v[88:91], v[44:47]\n"
      "v_add_u32 v131, s41, v131\n"
      "v_mfma_f32_16x16x32_bf16 v[48:51], v[64:67], v[92:95], v[48:51]\n"
      "v_add_u32 v132, s41, v132\n"
      "v_mfma_f32_16x16x32_bf16 v[52:55], v[68:71], v[92:95], v[52:55]\n"
      "v_add_u32 v133, s41, v133\n"
      "v_mfma_f32_16x16x32_bf16 v[56:59], v[72:75], v[92:95], v[56:59]\n"
      "v_add_u32 v129, s41, v129\n"
      "v_mfma_f32_16x16x32_bf16 v[60:63], v[76:79], v[92:95], v[60:63]\n"
      "s_waitcnt lgkmcnt(0)\n"
      "v_mfma_f32_16x16x32_bf16 v[0:3], v[96:99], v[112:115], v[0:3]\n"
      "v_mfma_f32_16x16x32_bf16 v[4:7], v[100:103], v[112:115], v[4:7]\n"
      "s_barrier\n"
      "v_mfma_f32_16x16x32_bf16 v[8:11], v[104:107], v[112:115], v[8:11]\n"
      "ds_read_b128 v[64:67], v128 offset:0\n"
      "v_mfma_f32_16x16x32_bf16 v[12:15], v[108:111], v[112:115], v[12:15]\n"
      "ds_read_b64_tr_b16 v[80:81], v130 offset:0\n"
      "v_mfma_f32_16x16x32_bf16 v[136:139], v[144:147], v[112:115], v[136:139]\n"
      "ds_read_b64_tr_b16 v[82:83], v130 offset:1024\n"
      "v_mfma_f32_16x16x32_bf16 v[16:19], v[96:99], v[116:119], v[16:19]\n"
      "ds_read_b128 v[68:71], v128 offset:2048\n"
      "v_mfma_f32_16x16x32_bf16 v[20:23], v[100:103], v[116:119], v[20:23]\n"
      "ds_read_b128 v[72:75], v128 offset:4096\n"
      "v_mfma_f32_16x16x32_bf16 v[24:27], v[104:107], v[116:119], v[24:27]\n"
      "ds_read_b128 v[76:79], v128 offset:6144\n"
      "v_mfma_f32_16x16x32_bf16 v[28:31], v[108:111], v[116:119], v[28:31]\n"
      "ds_read_b64_tr_b16 v[84:85], v131 offset:0\n"
      "v_mfma_f32_16x16x32_bf16 v[140:143], v[144:147], v[116:119], v[140:143]\n"
      "ds_read_b64_tr_b16 v[86:87], v131 offset:1024\n"
      "v_mfma_f32_16x16x32_bf16 v[32:35], v[96:99], v[120:123], v[32:35]\n"
      "ds_read_b64_tr_b16 v[88:89], v132 offset:0\n"
      "v_mfma_f32_16x16x32_bf16 v[36:39], v[100:103], v[120:123], v[36:39]\n"
      "ds_read_b64_tr_b16 v[90:91], v132 offset:1024\n"
      "v_mfma_f32_16x16x32_bf16 v[40:43], v[104:107], v[120:123], v[40:43]\n"
      "ds_read_b64_tr_b16 v[92:93], v133 offset:0\n"
      "v_mfma_f32_16x16x32_bf16 v[44:47], v[108:111], v[120:123], v[44:47]\n"
      "ds_read_b64_tr_b16 v[94:95], v133 offset:1024\n"
      "v_mfma_f32_16x16x32_bf16 v[48:51], v[96:99], v[124:127], v[48:51]\n"
      "s_add_u32 s40, s40, 1\n"
      "s_and_b32 s42, s40, 3\n"
      "v_mfma_f32_16x16x32_bf16 v[52:55], v[100:103], v[124:127], v[52:55]\n"
      "s_cmp_eq_u32 s42, 3\n"
      "s_cselect_b32 s41, 0xfffe8000, s45\n"
      "v_mfma_f32_16x16x32_bf16 v[56:59], v[104:107], v[124:127], v[56:59]\n"
      "s_cmp_lg_u32 s40, 10\n"
      "v_mfma_f32_16x16x32_bf16 v[60:63], v[108:111], v[124:127], v[60:63]\n"
      "s_waitcnt lgkmcnt(0)\n"
      "s_cbranch_scc1 Lg_%=\n"
      "s_nop 7\n"
      G_OPERANDS);
  }
  f32x4 m4[4], r4[4], c4[4];
#pragma unroll
  for (int rt = 0; rt < 4; ++rt) {
    if (ORI == 0) { m4[rt] = lds_ld<float>(sa + 64 * rt); r4[rt] = lds_ld<float>(sa + 512 + 64 * rt); }
    else { m4[rt] = lds_ld<f32x4>(sa + 64 * rt); r4[rt] = lds_ld<f32x4>(sa + 512 + 64 * rt); }
  }
#pragma unroll
  for (int j = 0; j < 2; ++j) {
    if (ORI == 0) { c4[j] = f32x4{cs[4 * j], cs[4 * j + 1], cs[4 * j + 2], cs[4 * j + 3]}; if (l15 == 0) lds_st<f32x4>(ca + 128 * rh + 64 * j, c4[j]); }
    else { c4[j] = cs[4 * j]; if (g == 0) lds_st<float>(ca + 128 * rh + 64 * j, cs[4 * j]); }
  }
  const unsigned tstep = ct < 5 ? 2560 : PT;
  auto frag = [&](int j, int t) {
    const f32x16& ac = acc[j]; const int o = 8 * t;
    const f32x4 v0 = (f32x4{ac[o], ac[o + 1], ac[o + 2], ac[o + 3]} - m4[2 * t] * c4[j]) * r4[2 * t], v1 = (f32x4{ac[o + 4], ac[o + 5], ac[o + 6], ac[o + 7]} - m4[2 * t + 1] * c4[j]) * r4[2 * t + 1];
    const unsigned a0 = pk2(v0[0], v0[1]), a1 = pk2(v0[2], v0[3]), b0 = pk2(v1[0], v1[1]), b1 = pk2(v1[2], v1[3]);
    const auto s0 = ORI == 0 ? __builtin_amdgcn_permlane16_swap(a0, b0, false, false) : __builtin_amdgcn_permlane32_swap(a0, b0, false, false);
    const auto s1 = ORI == 0 ? __builtin_amdgcn_permlane16_swap(a1, b1, false, false) : __builtin_amdgcn_permlane32_swap(a1, b1, false, false);
    return u32x4{s0[0], s1[0], s0[1], s1[1]}; };
  auto emit = [&](int j) { __builtin_amdgcn_raw_buffer_store_b128(frag(j, 0), rs_ws, dst[j], 0, SC1); __builtin_amdgcn_raw_buffer_store_b128(frag(j, 1), rs_ws, dst[j] + tstep, 0, SC1); };
  emit(0);
  BAR();
#pragma unroll
  for (int j = 2; j < 4; ++j) { if (ORI == 0) c4[j] = lds_ld<f32x4>(ca + 128 * (1 - rh) + 64 * (j - 2)); else c4[j] = lds_ld<float>(ca + 128 * (1 - rh) + 64 * (j - 2)); }
  emit(1); emit(2); emit(3);
  DRAIN();
  unsigned old = 0;
  if (lane == 0) old = __hip_atomic_fetch_add((LAS unsigned*)(uintptr_t)L_PUB, 1u, RLX_WG);
  if (uni(old) == 3 && lane == 0) __hip_atomic_store((GAS u64*)(ws + WS_GF + (ct * 16 + grb) * FS), make_tag(nonce), RLX_AGENT);
}

DI void stats_phase(const char* x, const char* wr, int lane, int w, int row0, __amdgpu_buffer_rsrc_t rs_ws, u64 nonce) {
  constexpr unsigned L_W = 147456, L_ONE = L_W + 1280;
  const int l15 = lane & 15, g = lane >> 4;
  const unsigned ri = lane >> 3, ph = lane & 7, v0 = ri * 1280 + ((ph ^ (ri >> 1)) << 4), v1 = (8 + ri) * 1280 + ((ph ^ (4 | (ri >> 1))) << 4), base = w * 18432;
  const char* xb = x + row0 * 1280;
  dma(wr, lane * 16, L_W); dma_masked(wr + 1024, lane * 16, L_W + 1024, 0xFFFFull);
#pragma unroll
  for (int k = 0; k < 9; ++k) { dma(xb + k * 128, v0, base + k * 2048); dma(xb + k * 128, v1, base + k * 2048 + 1024); }
  lds_st<u32x4>(L_ONE + lane * 16, u32x4{ONE2, ONE2, ONE2, ONE2});
  if (lane < 16) lds_st<u32x4>(L_ONE + 1024 + lane * 16, u32x4{ONE2, ONE2, ONE2, ONE2});
  const unsigned a0 = base + l15 * 128 + ((g ^ (l15 >> 1)) << 4), a1 = a0 ^ 64, wa = (l15 == 0 ? L_ONE : L_W) + g * 16;
  f32x4 s1[2] = {}, s2[2] = {};
  auto lines = [&](int k0, int k1) {
    bf16x8 f[4][4];
#pragma unroll
    for (int k = k0; k < k1; ++k) { f[k - k0][0] = lds_ld<bf16x8>(a0 + (k % 9) * 2048); f[k - k0][1] = lds_ld<bf16x8>(a1 + (k % 9) * 2048);
      f[k - k0][2] = lds_ld<bf16x8>(wa + k * 128); f[k - k0][3] = lds_ld<bf16x8>(wa + k * 128 + 64); }
    if (k0 == 0) { asm volatile("s_waitcnt lgkmcnt(0)" ::: "memory"); dma(xb + 9 * 128, v0, base); dma(xb + 9 * 128, v1, base + 1024); }
#pragma unroll
    for (int k = 0; k < k1 - k0; ++k)
#pragma unroll
      for (int i = 0; i < 2; ++i) { s1[i] = MFMA16(f[k][2 + i], f[k][i], s1[i]); s2[i] = MFMA16(f[k][i], f[k][i], s2[i]); } };
  PACE(12);
  float cw = 0.f;
#pragma unroll
  for (int i = 0; i < 5; ++i) { const unsigned u = lds_ld<unsigned>(L_W + lane * 4 + i * 256); cw += blo(u) + bhi(u); }
  lines(0, 3); PACE(8); lines(3, 6); PACE(0); lines(6, 10);
  cw += __int_as_float(__builtin_amdgcn_mov_dpp(__float_as_int(cw), 0xB1, 0xf, 0xf, true)); cw += __int_as_float(__builtin_amdgcn_mov_dpp(__float_as_int(cw), 0x4E, 0xf, 0xf, true));
  cw += __int_as_float(__builtin_amdgcn_mov_dpp(__float_as_int(cw), 0x141, 0xf, 0xf, true)); cw += __int_as_float(__builtin_amdgcn_mov_dpp(__float_as_int(cw), 0x128, 0xf, 0xf, true));
  cw = xsum(cw);
  const f32x4 t1 = s1[0] + s1[1], t2 = s2[0] + s2[1];
  const int e = l15 & 3; const float d = e == 0 ? t2[0] : e == 1 ? t2[1] : e == 2 ? t2[2] : t2[3];
  const float mean = t1[0] * (1.0f / 640.0f), var = fmaxf(xsum(g == (l15 >> 2) ? d : 0.f) * (1.0f / 640.0f) - mean * mean, 0.f);
  const float mu = bfr(mean), sg = bfr(__builtin_amdgcn_sqrtf(bfr(bfr(var) + 1e-5f))), r = (t1[1] - mu * cw) * __builtin_amdgcn_rcpf(sg);
  const unsigned tag = (unsigned)make_tag(nonce);
  if (lane < 16) {
    __builtin_amdgcn_raw_buffer_store_b64(u32x2{pk2(mu, sg), tag}, rs_ws, WS_ST + (row0 + lane) * 8, 0, SC1);
    __builtin_amdgcn_raw_buffer_store_b64(u32x2{__float_as_uint(r), tag}, rs_ws, WS_R + (row0 + lane) * 8, 0, SC1);
  }
}

DI void select_phase(const char* x, char* ws, int lane, int w, u64 tag, __amdgpu_buffer_rsrc_t rs_ws) {
  constexpr unsigned L_KEY = 0, L_WMAX = 8192, L_CNT = 8224;
  u32x4 r0, r1;
#pragma unroll 1
  for (unsigned spin = 0; spin < SPIN_CAP; ++spin) {
    asm volatile("" ::: "memory");
    r0 = __builtin_amdgcn_raw_buffer_load_b128(rs_ws, WS_R + (w * 64 + lane) * 32, 0, SC1); r1 = __builtin_amdgcn_raw_buffer_load_b128(rs_ws, WS_R + (w * 64 + lane) * 32 + 16, 0, SC1);
    if (__all(r0[1] == (unsigned)tag && r0[3] == (unsigned)tag && r1[1] == (unsigned)tag && r1[3] == (unsigned)tag)) break;
  }
  unsigned key[4] = {r0[0], r0[2], r1[0], r1[2]};
#pragma unroll
  for (int e = 0; e < 4; ++e) key[e] = ((key[e] ^ ((unsigned)((int)key[e] >> 31) | 0x80000000u)) & ~2047u) | (2047u - (unsigned)(w * 256 + lane * 4 + e));
  unsigned m = max(max(key[0], key[1]), max(key[2], key[3]));
  m = max(m, (unsigned)__builtin_amdgcn_mov_dpp((int)m, 0xB1, 0xf, 0xf, true)); m = max(m, (unsigned)__builtin_amdgcn_mov_dpp((int)m, 0x4E, 0xf, 0xf, true));
  m = max(m, (unsigned)__builtin_amdgcn_mov_dpp((int)m, 0x141, 0xf, 0xf, true)); m = max(m, (unsigned)__builtin_amdgcn_mov_dpp((int)m, 0x128, 0xf, 0xf, true));
  { const auto t = __builtin_amdgcn_permlane16_swap(m, m, false, false); m = max(t[0], t[1]); const auto u = __builtin_amdgcn_permlane32_swap(m, m, false, false); m = max(u[0], u[1]); }
  if (lane == 0) { lds_st<unsigned>(L_WMAX + w * 4, m); if (w == 0) lds_st<unsigned>(L_CNT, 0u); }
  BAR();
  const u32x4 a = lds_ld<u32x4>(L_WMAX), c = lds_ld<u32x4>(L_WMAX + 16);
  const unsigned thr = min(min(min(a[0], a[1]), min(a[2], a[3])), min(min(c[0], c[1]), min(c[2], c[3])));
#pragma unroll
  for (int e = 0; e < 4; ++e)
    if (key[e] >= thr) lds_st<unsigned>(L_KEY + __hip_atomic_fetch_add((LAS unsigned*)(uintptr_t)L_CNT, 1u, RLX_WG) * 4, key[e]);
  BAR();
  if (w == 0) {
    const int n = uni(lds_ld<unsigned>(L_CNT));
#pragma unroll 1
    for (int c0 = 0; c0 < n; c0 += 64) {
      const unsigned mine = c0 + lane < n ? lds_ld<unsigned>(L_KEY + (c0 + lane) * 4) : 0u;
      int rank = 0;
#pragma unroll 4
      for (int i = 0; i < n; ++i) rank += lds_ld<unsigned>(L_KEY + i * 4) > mine;
      if (c0 + lane < n && rank < 8) lds_st<unsigned>(L_WMAX + rank * 4, 2047u - (mine & 2047u));
    }
  }
  BAR();
  const unsigned row = lds_ld<unsigned>(L_WMAX + w * 4) & 2047u, mine = lds_ld<unsigned>(L_WMAX + (lane & 7) * 4);
  const u32x4 xa = *(const GAS u32x4*)(x + row * 1280 + lane * 16), xb = *(const GAS u32x4*)(x + row * 1280 + 1024 + (lane & 15) * 16);
  const unsigned sp = wait_granule(ws, WS_ST + row * 8, (unsigned)tag);
  const float mu = blo(sp), rs = __builtin_amdgcn_rcpf(bhi(sp));
  u32x4 na, nb;
#pragma unroll
  for (int i = 0; i < 4; ++i) { na[i] = ln2(xa[i], mu, rs); nb[i] = ln2(xb[i], mu, rs); }
  __builtin_amdgcn_raw_buffer_store_b128(na, rs_ws, WS_XN + lane * 128 + w * 16, 0, SC1);
  __builtin_amdgcn_raw_buffer_store_b128(nb, rs_ws, WS_XN + (64 + (lane & 15)) * 128 + w * 16, 0, SC1);
  BARV(0);
  if (w == 0) {
    __hip_atomic_store((GAS u64*)(ws + WS_SEL + (lane >> 3) * 128 + (lane & 7) * 8), (tag << 32) | mine, RLX_AGENT);
    __hip_atomic_store((GAS u64*)(ws + WS_SEL + 1024 + (lane >> 3) * 128 + (lane & 7) * 8), (tag << 32) | mine, RLX_AGENT);
  }
}

struct Fin { u32x4 xv[3]; f32x2 gv[3][4]; unsigned st[3], pos[3]; };
DI unsigned fin_off(unsigned pos, int rb64, int cs) { return (rb64 * 64 + (pos >> 4)) * 1280 + cs * 160 + (pos & 15) * 16; }
template <bool UPD> DI void fin_gate(Fin& f, int k, u32x4 y) {
  const float mu = blo(f.st[k]), rs = __builtin_amdgcn_rcpf(bhi(f.st[k]));
#pragma unroll
  for (int i = 0; i < 4; ++i) {
    unsigned n = ln2(f.xv[k][i], mu, rs);
    if (UPD) n = pk2(blo(n) + blo(y[i]), bhi(n) + bhi(y[i]));
    const unsigned g = pk2(blo(n) * sigm(blo(n)), bhi(n) * sigm(bhi(n)));
    f.gv[k][i] = f32x2{blo(g), bhi(g)};
    asm volatile("" : "+v"(f.gv[k][i]));
  }
}
DI u64 fin_load(Fin& f, const char* x, const char* ws, int hw, int rb64, int cs, int lane) {
#pragma unroll
  for (int k = 0; k < 3; ++k) {
    const int t = k < 2 ? 64 * k + lane : 128 + (lane & 31), r = t / 10;
    f.pos[k] = (hw * 16 + r) * 16 + (t - 10 * r);
    f.xv[k] = *(const GAS u32x4*)(x + fin_off(f.pos[k], rb64, cs));
  }
  return __hip_atomic_load((GAS u64*)(ws + WS_ST + (rb64 * 64 + hw * 16 + (lane & 15)) * 8), RLX_AGENT);
}
DI void fin_prep(Fin& f, u64 st, const char* ws, int hw, int rb64, int lane, unsigned tag) {
  const unsigned sp = __all((unsigned)(st >> 32) == tag) ? (unsigned)st : wait_granule(ws, WS_ST + (rb64 * 64 + hw * 16 + (lane & 15)) * 8, tag);
#pragma unroll
  for (int k = 0; k < 3; ++k) { f.st[k] = __builtin_amdgcn_ds_bpermute(f.pos[k] >> 2 & 60, sp); fin_gate<false>(f, k, f.xv[k]);
    asm volatile("" : "+v"(f.xv[k]), "+v"(f.st[k]), "+v"(f.pos[k])); }
}
DI bool routed_here(int rb64) { return __any((int)(lds_ld<unsigned>(L_SEL + (fresh_lane() & 7) * 4) >> 6) == rb64); }
DI void fin_routed(Fin& f, int rb64, int cs, __amdgpu_buffer_rsrc_t rs_ws) {
  const u32x4 s0 = lds_ld<u32x4>(L_SEL), s1 = lds_ld<u32x4>(L_SEL + 16);
#pragma unroll
  for (int k = 0; k < 3; ++k) {
    const unsigned row = rb64 * 64 + (f.pos[k] >> 4);
    int ts = -1;
#pragma unroll
    for (int i = 0; i < 4; ++i) { ts = s0[i] == row ? i : ts; ts = s1[i] == row ? i + 4 : ts; }
    if (ts >= 0) fin_gate<true>(f, k, __builtin_amdgcn_raw_buffer_load_b128(rs_ws, WS_Y + ts * 1280 + cs * 160 + (f.pos[k] & 15) * 16, 0, SC1));
  }
}
DI void fin_store(const Fin& f, int rb64, int cs, char* out) {
  unsigned la[3], off[3]; f32x2 xf[3][4];
#pragma unroll
  for (int k = 0; k < 3; ++k) {
    la[k] = L_FX + (f.pos[k] >> 4) * 336 + (f.pos[k] & 15) * 32; off[k] = fin_off(f.pos[k], rb64, cs);
    asm volatile("" : "+v"(la[k]), "+v"(off[k]));
#pragma unroll
    for (int i = 0; i < 4; ++i) { xf[k][i] = f32x2{blo(f.xv[k][i]), bhi(f.xv[k][i])}; asm volatile("" : "+v"(xf[k][i])); }
  }
  BAR();
  f32x4 a[3][4];
#pragma unroll
  for (int k = 0; k < 3; ++k)
#pragma unroll
    for (int i = 0; i < 4; ++i) a[k][i] = lds_ld<f32x4>(la[k] + (i & 1) * 16 + (i >> 1) * 21504);
  __builtin_amdgcn_sched_barrier(0);
#pragma unroll
  for (int k = 0; k < 3; ++k) {
    u32x4 o;
#pragma unroll
    for (int i = 0; i < 4; ++i) {
      const f32x2 v = xf[k][i] + (f32x2{a[k][i >> 1][2 * (i & 1)], a[k][i >> 1][2 * (i & 1) + 1]} + f32x2{a[k][2 + (i >> 1)][2 * (i & 1)], a[k][2 + (i >> 1)][2 * (i & 1) + 1]}) * f.gv[k][i];
      o[i] = pk2(v[0], v[1]);
    }
    *(GAS u32x4*)(out + off[k]) = o;
  }
}

__global__ __launch_bounds__(768, 3) void fused_kernel(const char* __restrict__ x, const char* __restrict__ wqkv, const char* __restrict__ wo, const char* __restrict__ wr,
                                                       const char* __restrict__ wg, const char* __restrict__ wu, const char* __restrict__ wd, char* __restrict__ out, char* ws) {
  __shared__ __attribute__((aligned(16))) char lds[L_END];
  const int w = uni(threadIdx.x >> 6);
  const int b = blockIdx.x, xcd = b & 7, j = b >> 3;
  const int rb = (xcd & 1) * 8 + (j & 7), hp = (xcd >> 1) * 4 + (j >> 3);
  const int rb64 = xcd * 4 + (j & 3), cs = j >> 2;
  const int grb = (xcd >> 1) * 4 + (j & 3), ct = (xcd & 1) * 8 + (j >> 2);
  const __amdgpu_buffer_rsrc_t rs_ws = __builtin_amdgcn_make_buffer_rsrc(ws, 0, WS_END, 0x00020000);
  if (uni((unsigned)(uintptr_t)(LAS char*)lds) != 0) return;
  u64 pc0; unsigned code_left;
  asm volatile("s_getpc_b64 %0\nLpc_%=:\n\ts_mov_b32 %1, Lfused_tail-Lpc_%=" : "=s"(pc0), "=s"(code_left));
#define LOAD_NONCE() unsigned nz = WS_NONCE; asm volatile("" : "+v"(nz)); const u64 nonce = __hip_atomic_load((GAS u64*)(ws + nz), RLX_AGENT)

  const int u = xcd * 24 + j, v = xcd * 5 + (j - 24);
  if (w >= 8) {
    const int lane = fresh_lane();
    LOAD_NONCE();
    __builtin_amdgcn_s_setprio(3);
    if (w == 8 && lane < 20) lds_st<unsigned>(L_PUB + lane * 4, 0u);
    Fin fin; u64 st;
    if (ct < 15) {
      unsigned vo[8]; const unsigned ri = lane >> 3, ph = lane & 7;
      const char* base; unsigned step;
      if (w < 10) {
        base = x + (grb * 128 + (w - 8) * 64) * 1280; step = 128;
#pragma unroll
        for (int i = 0; i < 8; ++i) vo[i] = (8 * i + ri) * 1280 + ((ph ^ (((i & 1) << 2) | (ri >> 1))) << 4) + 4096 - i * 1024;
      } else {
        base = wqkv + ct * 256 + (w - 10) * 128; step = 64 * 3840;
#pragma unroll
        for (int i = 0; i < 8; ++i) vo[i] = (8 * (ri >> 2) + (ri & 3) + 16 * (i >> 1) + 4 * (i & 1)) * 3840 + ((ph ^ (((ri >> 1) & 3) << 1)) << 4) + 4096 - i * 1024;
      }
      int cc = (((j >> 2) + (j & 3)) & 1) * 5;
      dma8(base + cc * step, vo, (w - 8) * 8192);
#pragma unroll 1
      for (int c = 1; c < 10; ++c) { cc = cc == 9 ? 0 : cc + 1; dma8(base + cc * step, vo, (c & 3) * 32768u + (w - 8) * 8192); BARV(8); }
      BARV(0);
      st = fin_load(fin, x, ws, w - 8, rb64, cs, lane);
      BAR(); BAR();
    } else {
      st = fin_load(fin, x, ws, w - 8, rb64, cs, lane);
      BAR();
      if (b == 255) { BAR(); BAR(); BAR(); BAR(); }
    }
    DRAIN();
    const u64 tag = make_tag(nonce);
    fin_prep(fin, st, ws, w - 8, rb64, lane, (unsigned)tag);
    if (w >= 9) {
      const int hw = w - 9;
      const unsigned e = lane >> 3, ph = lane & 7, pr = lane >> 1, jj = pr >> 3, e2 = (pr & 7) ^ ((jj & 1) << 2);
      unsigned va[2][2], vb[5];
#pragma unroll
      for (int par = 0; par < 2; ++par)
#pragma unroll
        for (int dl = 0; dl < 2; ++dl) va[par][dl] = (e + (e >= 4 ? (dl ? 16 : 4) : 0)) * 1280 + ((ph ^ ((((e >> 1) & 1) | (par << 1)) << 1)) << 4);
#pragma unroll
      for (int i = 0; i < 5; ++i) { const unsigned P = 4 * i + jj, hq = P / 5, pi = P - 5 * hq, r1 = pi < 2 ? 4 * pi : 8 + 4 * pi;
        vb[i] = (40 * hq + r1 + e2 + (e2 >= 4 ? (pi == 2 ? 16 : 4) : 0)) * 1280 + (lane & 1) * 16; }
      wait_lds<4>(L_READY, 1); __builtin_amdgcn_s_sleep(60);
      if (j < 24) {
#pragma unroll 1
        for (int i = hw; i < 20; i += 3) { dma((i & 1 ? wu : wg) + u * 16 + (i >> 1) * (64 * 3072), lane * 3072, (i & 1 ? L_UP : L_GATE) + (i >> 1) * 1024); PACE(0); }
      } else if (j < 29) {
        const unsigned voff = (pr ^ (((pr >> 3) & 1) << 2)) * 1280 + (lane & 1) * 16;
#pragma unroll 1
        for (int i = hw; i < 48; i += 3) { dma(wd + v * 32 + i * (32 * 1280), voff, L_WD + i * 1024); PACE(1); }
      }
      DRAIN();
      if (lane == 0) __hip_atomic_fetch_add((LAS unsigned*)(uintptr_t)L_WOK, 1u, RLX_WG);
#pragma unroll 1
      for (int it = 3 * hw; it < min(3 * hw + 3, 8); ++it) {
#pragma unroll
        for (int i = 0; i < 10; ++i) { const int r1 = (i % 5 == 0) ? 0 : (i % 5 == 1) ? 4 : (i % 5 == 2) ? 16 : (i % 5 == 3) ? 20 : 24;
          dma(wo + cs * 160 + (it * 80 + (i / 5) * 40 + r1) * 1280, va[i & 1][i % 5 == 2], L_WOA + (it * 10 + i) * 1024); PACE(2); }
      }
      if (hw == 2) {
#pragma unroll 1
        for (int k = 0; k < 4; ++k) {
#pragma unroll
          for (int i = 0; i < 5; ++i) { dma(wo + cs * 160 + 128 + k * (160 * 1280), vb[i], L_WOB + (k * 5 + i) * 1024); PACE(1); }
        }
      }
      DRAIN();
      if (lane == 0) __hip_atomic_fetch_add((LAS unsigned*)(uintptr_t)L_WOD, 1u, RLX_WG);
    } else {
      const int l15 = lane & 15, g = lane >> 4, q = l15 >> 2, p = lane & 3;
      wait_lds<6>(L_WOK, 3);
      const unsigned idx = wait_granule<4>(ws, WS_SEL + (b & 15) * 128 + (lane & 7) * 8, (unsigned)tag) & 2047u;
      if (lane < 8) lds_st<unsigned>(L_SEL + lane * 4, idx);
      if (j < 24) {
        u32x4 xf[10];
#pragma unroll
        for (int s = 0; s < 10; ++s) xf[s] = __builtin_amdgcn_raw_buffer_load_b128(rs_ws, WS_XN + (l15 >> 3) * 512 + g * 128 + (lane & 7) * 16 + (s & 3) * 1024, (s >> 2) * 4096, SC1);
        const unsigned aa = (p >> 1 ? L_UP : L_GATE) + (8 * g + q) * 16 + (p & 1) * 8;
        f32x4 acc2[2] = {};
#pragma unroll
        for (int s0 = 0; s0 < 20; s0 += 10) {
          bf16x8 af[10];
#pragma unroll
          for (int s = 0; s < 10; ++s) af[s] = lds_tr8(aa + (s0 + s) * 512, aa + (s0 + s) * 512 + 64);
          __builtin_amdgcn_sched_barrier(0);
#pragma unroll
          for (int s = 0; s < 10; ++s) acc2[s & 1] = MFMA16(af[s], __builtin_bit_cast(bf16x8, xf[(s0 + s) >> 1]), acc2[s & 1]);
        }
        float hv[4];
#pragma unroll
        for (int r = 0; r < 4; ++r) {
          const float val = bfr(acc2[0][r] + __int_as_float(__builtin_amdgcn_mov_dpp(__float_as_int(acc2[1][r]), 0x128, 0xf, 0xf, true)));
          const auto gu = __builtin_amdgcn_permlane32_swap(__float_as_uint(val), __float_as_uint(val), false, false);
          const float gt = __uint_as_float(gu[0]);
          hv[r] = bfr(gt * sigm(gt)) * __uint_as_float(gu[1]);
        }
        if (lane < 32 && l15 < 8) __builtin_amdgcn_raw_buffer_store_b64(u32x2{pk2(hv[0], hv[1]), pk2(hv[2], hv[3])}, rs_ws, WS_H + u * 128 + l15 * 16 + g * 8, 0, SC1);
        DRAIN();
        if (lane == 0) __hip_atomic_store((GAS u64*)(ws + WS_HF + u * FS), tag, RLX_AGENT);
      } else if (j < 29) {
        const float rr = bfr(__uint_as_float(wait_granule(ws, WS_R + idx * 8, (unsigned)tag)));
        wait_flags<4>(ws, {WS_HF + lane * FS, WS_HF + (64 + lane) * FS, WS_HF + (128 + lane) * FS}, tag);
        const unsigned a0 = L_WD + (8 * g + (((g & 1) << 2) | q)) * 32 + 8 * p, a1 = a0 ^ 128, ho = WS_H + (l15 >> 3) * 512 + g * 128 + (lane & 7) * 16;
        f32x4 acc2[2] = {};
        u32x4 hf[8];
#pragma unroll
        for (int s = 0; s < 8; ++s) hf[s] = __builtin_amdgcn_raw_buffer_load_b128(rs_ws, ho + (s & 3) * 1024, (s >> 2) * 4096, SC1);
#pragma unroll 1
        for (int s0 = 0; s0 < 48; s0 += 16) {
          const unsigned nx = s0 < 32 ? ho + (s0 + 16) * 512 : 0x40000000u;
#pragma unroll
          for (int s8 = 0; s8 < 16; s8 += 8) {
            bf16x8 af[8];
#pragma unroll
            for (int s = 0; s < 8; ++s) af[s] = lds_tr8(a0 + (s0 + s8 + s) * 1024, a1 + (s0 + s8 + s) * 1024);
            __builtin_amdgcn_sched_barrier(0);
#pragma unroll
            for (int s = 0; s < 8; ++s) acc2[s & 1] = MFMA16(af[s], __builtin_bit_cast(bf16x8, hf[(s8 + s) >> 1]), acc2[s & 1]);
#pragma unroll
            for (int s = s8 >> 1; s < (s8 >> 1) + 4; ++s) hf[s] = __builtin_amdgcn_raw_buffer_load_b128(rs_ws, nx + (s & 3) * 1024, (s >> 2) * 4096, SC1);
          }
        }
        f32x4 acc;
#pragma unroll
        for (int r = 0; r < 4; ++r) acc[r] = acc2[0][r] + __int_as_float(__builtin_amdgcn_mov_dpp(__float_as_int(acc2[1][r]), 0x128, 0xf, 0xf, true));
        const float rsel = bfr(sigm(rr));
        if (l15 < 8) __builtin_amdgcn_raw_buffer_store_b64(u32x2{pk2(bfr(acc[0]) * rsel, bfr(acc[1]) * rsel), pk2(bfr(acc[2]) * rsel, bfr(acc[3]) * rsel)}, rs_ws, WS_Y + l15 * 1280 + v * 32 + g * 8, 0, SC1);
        DRAIN();
        if (lane == 0) __hip_atomic_store((GAS u64*)(ws + WS_YF + v * FS), tag, RLX_AGENT);
      }
      asm volatile("s_waitcnt lgkmcnt(0)" ::: "memory");
      if (lane == 0) __hip_atomic_store((LAS unsigned*)(uintptr_t)L_AUX, 1u, RLX_WG);
      if (__any((int)(idx >> 6) == rb64)) wait_flags<4>(ws, {WS_YF + (cs * 5 + (lane < 5 ? lane : 0)) * FS}, tag);
      if (lane == 0) __hip_atomic_store((LAS unsigned*)(uintptr_t)L_AUX, 2u, RLX_WG);
    }
    wait_lds<8>(L_PAIR, 1);
    if (w == 11) {
      u64 f = lane < 16 ? ~tag : tag;
#pragma unroll 1
      for (unsigned spin = 0; spin < SPIN_CAP; ++spin) {
        if (f != tag) f = __hip_atomic_load((GAS u64*)(ws + WS_OF + ((rb64 >> 1) * 16 + lane) * FS), RLX_AGENT);
        const unsigned m = spin + 1 < SPIN_CAP ? (unsigned)__ballot(f == tag) & 0xFFFFu : 0xFFFFu;
        if (lane == 0) __hip_atomic_store((LAS unsigned*)(uintptr_t)L_OMASK, m, RLX_WG);
        if (m == 0xFFFFu) break;
        __builtin_amdgcn_s_sleep(2);
      }
      if (cs == 0 && !(rb64 & 1) && lane == 0) __hip_atomic_store((GAS u64*)(ws + WS_NF + (rb64 >> 1) * FS), tag, RLX_AGENT);
    }
    __builtin_amdgcn_s_setprio(1);
    wait_lds<2>(L_AUX, 2);
    if (routed_here(rb64)) fin_routed(fin, rb64, cs, rs_ws);
    __builtin_amdgcn_s_setprio(3);
    if (b == 0 && w == 10) {
      wait_flags<4>(ws, {WS_NF + (lane & 15) * FS}, tag);
      if (lane == 0) __hip_atomic_store((GAS u64*)(ws + WS_NONCE), tag, RLX_AGENT);
    }
    fin_store(fin, rb64, cs, out);
    asm volatile("Lfused_tail:");
    return;
  }

  const int lane = fresh_lane();
  LOAD_NONCE();
  if (ct < 15) {
    if (w < 4) {
      if (ct < 10) g_phase<0>(ws, lane, w, ct, grb, rs_ws, nonce); else g_phase<1>(ws, lane, w, ct, grb, rs_ws, nonce);
    } else {
      unsigned code = 0;
      if (w == 6) code = *(const GAS unsigned*)(pc0 + min((unsigned)(j % 5 * 64 + lane) * 128, code_left - 4));
      BAR();
#pragma unroll 1
      for (int c = 0; c < 8; ++c) BAR();
      asm volatile("" :: "v"(code));
      u32x4 sv;
      if (w == 4) sv = __builtin_amdgcn_raw_buffer_load_b128(rs_ws, WS_ST + grb * 1024 + lane * 16, 0, SC1);
      BAR();
      if (w == 4) {
        const unsigned tg = (unsigned)make_tag(nonce);
#pragma unroll 1
        for (unsigned spin = 0; spin < SPIN_CAP && !__all(sv[1] == tg && sv[3] == tg); ++spin) {
          asm volatile("" ::: "memory");
          sv = __builtin_amdgcn_raw_buffer_load_b128(rs_ws, WS_ST + grb * 1024 + lane * 16, 0, SC1);
        }
        const float qs = ct < 5 ? 0.32259641f : 1.0f;
        lds_st<f32x2>(L_MU + lane * 8, f32x2{blo(sv[0]), blo(sv[2])});
        lds_st<f32x2>(L_RS + lane * 8, f32x2{__builtin_amdgcn_rcpf(bhi(sv[0])) * qs, __builtin_amdgcn_rcpf(bhi(sv[2])) * qs});
      }
      BAR(); BAR();
    }
  } else {
    stats_phase(x, wr, lane, w, grb * 128 + w * 16, rs_ws, nonce);
    BAR();
    if (b == 255) { select_phase(x, ws, lane, w, make_tag(nonce), rs_ws); }
  }
  if (w == 7) {
    const int lo = 5 * hp >> 4, hi = (5 * hp + 4) >> 4;
    const unsigned kvf = WS_GF + ((5 + 5 * (lane >> 5) + (lane & 16 ? hi : lo)) * 16 + (lane & 15)) * FS;
    if (ct < 15) wait_lds(L_PUB, 4);
    wait_flags<2>(ws, {kvf, WS_GF + ((lane & 1 ? hi : lo) * 16 + rb) * FS}, make_tag(nonce));
    if (lane == 0) __hip_atomic_store((LAS unsigned*)(uintptr_t)L_READY, 1u, RLX_WG);
  }
  const unsigned tag_lo = (unsigned)make_tag(nonce), tag_hi = (unsigned)(make_tag(nonce) >> 32);
  const int a = w & 1, qt = w >> 1;
  unsigned vk16 = a * 1024 + lane * 16, vk8 = 2048 + a * 8 + (lane & 31) * 16, vv = 2560 + a * 1280 + (lane >> 5) * 320 + (lane & 31) * 16;
  const unsigned kv0 = WS_KV + hp * 64 * PT;
  u32x4 q16[4] = {}; u32x2 q8[4] = {};
  for (int attempt = 0; attempt < 2; ++attempt) {
    if (attempt) {
      const int lane = fresh_lane(), r = lane & 31, h = lane >> 5;
      float mx[4] = {-__builtin_inff(), -__builtin_inff(), -__builtin_inff(), -__builtin_inff()};
#pragma unroll 1
      for (int t = 0; t < 16; ++t) {
        const u32x4 a16 = __builtin_amdgcn_raw_buffer_load_b128(rs_ws, vk16, kv0 + (t * 4 + qt) * PT, SC1); const u32x2 a8 = __builtin_amdgcn_raw_buffer_load_b64(rs_ws, vk8, kv0 + (t * 4 + qt) * PT, SC1);
#pragma unroll
        for (int qb = 0; qb < 4; ++qb) {
          f32x16 s = MFMA32(__builtin_bit_cast(bf16x8, a16), __builtin_bit_cast(bf16x8, q16[qb]), f32x16{});
          const u32x2 z8 = h ? u32x2{0u, 0u} : q8[qb];
          s = MFMA32K8(__builtin_bit_cast(bf16x4, a8), __builtin_bit_cast(bf16x4, z8), s);
          mx[qb] = fmaxf(mx[qb], max16(s));
        }
      }
#pragma unroll
      for (int qb = 0; qb < 4; ++qb) { const float m = xmax32(mx[qb]); if (h == 0) lds_st<float>(L_CMB + 4 * 12288 + ((a * 4 + qt) * 128 + qb * 32 + r) * 4, m); }
      asm volatile("s_waitcnt lgkmcnt(0)" ::: "memory");
      if (lane == 0) __hip_atomic_fetch_add((LAS unsigned*)(uintptr_t)L_MAXC, 1u, RLX_WG);
      wait_lds(L_MAXC, 8);
#pragma unroll
      for (int qb = 0; qb < 4; ++qb) {
        const unsigned ma = L_CMB + 4 * 12288 + (a * 512 + qb * 32 + r) * 4;
        const u32x2 sp = neg_split(fmaxf(fmaxf(lds_ld<float>(ma), lds_ld<float>(ma + 512)), fmaxf(lds_ld<float>(ma + 1024), lds_ld<float>(ma + 1536))));
        q8[qb] = h ? sp : q8[qb];
      }
    }
    if (w >= 4) __builtin_amdgcn_s_setprio(1); else __builtin_amdgcn_s_setprio(2);
    f32x16 o0, o1, o2, o3;
    {
      const u64 wb = (u64)ws;
      const u32x4 srd = {(unsigned)wb, (unsigned)(wb >> 32), WS_END, 0x00020000u};
      const int t0 = 4 * ((xcd & 1) * 8);
      unsigned soff = kv0 + (t0 + qt) * PT, sw = w < 4 ? 13u : 1000u, iters = 8;
      const unsigned send = kv0 + 64 * PT, rdy = L_READY, pre = (fresh_lane() & 31) == 20 ? ONE2 : 0u, qo = WS_Q + (hp * 64 + 4 * rb) * 2560;
      asm volatile(
      "s_mov_b32 s50, -1\n"
      "s_mov_b32 s51, 0\n"
      "s_mov_b32 s52, 0xfffff\n"
      "s_mov_b32 s53, 0xfffff\n"
      "v_mov_b32 v124, 0x3f803f80\n"
      "v_mov_b32 v125, 0x3f803f80\n"
      "v_mov_b32 v132, v152\n"
      "v_mov_b32 v133, v152\n"
      "v_mov_b32 v134, v152\n"
      "v_mov_b32 v135, v152\n"
      "v_mov_b32 v136, v152\n"
      "v_mov_b32 v137, v152\n"
      "v_mov_b32 v138, v152\n"
      "v_mov_b32 v139, v152\n"
      "v_mov_b32 v130, 0x3f803f80\n"
      "v_mov_b32 v131, 0x3f803f80\n"
      "v_mov_b32 v140, v152\n"
      "v_mov_b32 v141, v152\n"
      "v_mov_b32 v142, v152\n"
      "v_mov_b32 v143, v152\n"
      "v_mov_b32 v144, v152\n"
      "v_mov_b32 v145, v152\n"
      "v_mov_b32 v146, v152\n"
      "v_mov_b32 v147, v152\n"
      "v_mov_b32 v158, 0x3f803f80\n"
      "v_mov_b32 v159, 0x3f803f80\n"
      "s_not_b64 s[62:63], s[50:51]\n"
      "s_cmp_lg_u32 s54, 0\n"
      "s_cbranch_scc1 Lre_%=\n"
      "s_mov_b32 s49, 0x40000\n"
      "Lrdy_%=:\n"
      "ds_read_b32 v64, v151\n"
      "s_sub_u32 s49, s49, 1\n"
      "s_waitcnt lgkmcnt(0)\n"
      "v_readfirstlane_b32 s48, v64\n"
      "s_cmp_eq_u32 s49, 0\n"
      "s_cbranch_scc1 Lgo_%=\n"
      "s_cmp_lg_u32 s48, 0\n"
      "s_cbranch_scc1 Lgo_%=\n"
      "s_sleep 1\n"
      "s_branch Lrdy_%=\n"
      "Lgo_%=:\n"
      "buffer_load_dwordx4 v[154:157], v148, s[40:43], s55 offen sc1\n"
      "buffer_load_dwordx4 v[96:99], v148, s[40:43], s56 offen sc1\n"
      "buffer_load_dwordx4 v[120:123], v148, s[40:43], s44 offen sc1\n"
      "s_mov_b64 exec, s[50:51]\n"
      "buffer_load_dwordx2 v[158:159], v149, s[40:43], s55 offen sc1\n"
      "buffer_load_dwordx2 v[100:101], v149, s[40:43], s56 offen sc1\n"
      "buffer_load_dwordx2 v[124:125], v149, s[40:43], s44 offen sc1\n"
      "buffer_load_dwordx2 v[106:107], v149, s[40:43], s56 offen offset:2560 sc1\n"
      "s_mov_b64 exec, -1\n"
      "buffer_load_dwordx4 v[102:105], v148, s[40:43], s56 offen offset:2560 sc1\n"
      "s_mov_b64 exec, s[52:53]\n"
      "buffer_load_dwordx4 v[132:135], v150, s[40:43], s44 offen sc1\n"
      "buffer_load_dwordx4 v[136:139], v150, s[40:43], s44 offen offset:640 sc1\n"
      "s_mov_b64 exec, -1\n"
      "s_add_u32 s44, s44, 20480\n"
      "s_cmp_ge_u32 s44, s45\n"
      "s_cselect_b32 s48, 327680, 0\n"
      "s_sub_u32 s44, s44, s48\n"
      "buffer_load_dwordx4 v[108:111], v148, s[40:43], s57 offen sc1\n"
      "buffer_load_dwordx4 v[114:117], v148, s[40:43], s57 offen offset:2560 sc1\n"
      "s_mov_b64 exec, s[50:51]\n"
      "buffer_load_dwordx2 v[112:113], v149, s[40:43], s57 offen sc1\n"
      "buffer_load_dwordx2 v[118:119], v149, s[40:43], s57 offen offset:2560 sc1\n"
      "s_mov_b64 exec, -1\n"
      "buffer_load_dwordx4 v[126:129], v148, s[40:43], s44 offen sc1\n"
      "s_mov_b64 exec, s[50:51]\n"
      "buffer_load_dwordx2 v[130:131], v149, s[40:43], s44 offen sc1\n"
      "s_mov_b64 exec, -1\n"
      "s_waitcnt vmcnt(10)\n"
      "v_mfma_f32_32x32x16_bf16 v[0:15], v[154:157], v[96:99], 0\n"
      "v_mfma_f32_32x32x8bf16_1k v[0:15], v[158:159], v[100:101], v[0:15]\n"
      "v_mfma_f32_32x32x16_bf16 v[64:79], v[120:123], v[96:99], 0\n"
      "s_waitcnt vmcnt(8)\n"
      "v_mfma_f32_32x32x16_bf16 v[16:31], v[154:157], v[102:105], 0\n"
      "v_mfma_f32_32x32x8bf16_1k v[16:31], v[158:159], v[106:107], v[16:31]\n"
      "s_nop 7\n"
      "v_max3_f32 v0, v0, v1, v2\n"
      "v_max3_f32 v3, v3, v4, v5\n"
      "v_max3_f32 v0, v0, v3, v6\n"
      "s_mov_b64 exec, s[62:63]\n"
      "v_cvt_pk_bf16_f32 v100, -v0, 0\n"
      "s_mov_b64 exec, -1\n"
      "s_branch Lst_%=\n"
      "Lre_%=:\n"
      "s_mov_b64 s[62:63], 0\n"
      "buffer_load_dwordx4 v[120:123], v148, s[40:43], s44 offen sc1\n"
      "s_mov_b64 exec, s[50:51]\n"
      "buffer_load_dwordx2 v[124:125], v149, s[40:43], s44 offen sc1\n"
      "s_mov_b64 exec, -1\n"
      "s_mov_b64 exec, s[52:53]\n"
      "buffer_load_dwordx4 v[132:135], v150, s[40:43], s44 offen sc1\n"
      "buffer_load_dwordx4 v[136:139], v150, s[40:43], s44 offen offset:640 sc1\n"
      "s_mov_b64 exec, -1\n"
      "s_add_u32 s44, s44, 20480\n"
      "s_cmp_ge_u32 s44, s45\n"
      "s_cselect_b32 s48, 327680, 0\n"
      "s_sub_u32 s44, s44, s48\n"
      "buffer_load_dwordx4 v[126:129], v148, s[40:43], s44 offen sc1\n"
      "s_mov_b64 exec, s[50:51]\n"
      "buffer_load_dwordx2 v[130:131], v149, s[40:43], s44 offen sc1\n"
      "s_mov_b64 exec, -1\n"
      "s_waitcnt vmcnt(4)\n"
      "v_mfma_f32_32x32x16_bf16 v[64:79], v[120:123], v[96:99], 0\n"
      "Lst_%=:\n"
      "v_mfma_f32_32x32x8bf16_1k v[64:79], v[124:125], v[100:101], v[64:79]\n"
      "v_max3_f32 v16, v16, v17, v18\n"
      "v_max3_f32 v19, v19, v20, v21\n"
      "v_max3_f32 v16, v16, v19, v22\n"
      "s_mov_b64 exec, s[62:63]\n"
      "v_cvt_pk_bf16_f32 v106, -v16, 0\n"
      "s_mov_b64 exec, -1\n"
      "s_nop 5\n"
      "v_exp_f32 v64, v64\n"
      "v_exp_f32 v65, v65\n"
      "v_exp_f32 v66, v66\n"
      "s_mov_b64 exec, s[52:53]\n"
      "buffer_load_dwordx4 v[140:143], v150, s[40:43], s44 offen sc1\n"
      "buffer_load_dwordx4 v[144:147], v150, s[40:43], s44 offen offset:640 sc1\n"
      "s_mov_b64 exec, -1\n"
      "v_exp_f32 v67, v67\n"
      "s_add_u32 s44, s44, 20480\n"
      "v_exp_f32 v68, v68\n"
      "s_cmp_ge_u32 s44, s45\n"
      "v_exp_f32 v69, v69\n"
      "s_cselect_b32 s48, 327680, 0\n"
      "v_mfma_f32_32x32x16_bf16 v[80:95], v[120:123], v[102:105], 0\n"
      "v_exp_f32 v70, v70\n"
      "s_sub_u32 s44, s44, s48\n"
      "v_exp_f32 v71, v71\n"
      "v_cvt_pk_bf16_f32 v64, v64, v65\n"
      "v_cvt_pk_bf16_f32 v65, v66, v67\n"
      "v_cvt_pk_bf16_f32 v66, v68, v69\n"
      "v_cvt_pk_bf16_f32 v67, v70, v71\n"
      "v_mfma_f32_32x32x8bf16_1k v[80:95], v[124:125], v[106:107], v[80:95]\n"
      "v_exp_f32 v72, v72\n"
      "v_exp_f32 v73, v73\n"
      "v_exp_f32 v74, v74\n"
      "v_exp_f32 v75, v75\n"
      "v_exp_f32 v76, v76\n"
      "s_waitcnt vmcnt(4)\n"
      "v_mfma_f32_32x32x16_bf16 v[0:15], v[132:135], v[64:67], 0\n"
      "v_exp_f32 v77, v77\n"
      "v_exp_f32 v78, v78\n"
      "v_exp_f32 v79, v79\n"
      "v_cvt_pk_bf16_f32 v72, v72, v73\n"
      "v_mfma_f32_32x32x16_bf16 v[32:47], v[154:157], v[108:111], 0\n"
      "v_cvt_pk_bf16_f32 v73, v74, v75\n"
      "v_cvt_pk_bf16_f32 v74, v76, v77\n"
      "v_cvt_pk_bf16_f32 v75, v78, v79\n"
      "v_mfma_f32_32x32x8bf16_1k v[32:47], v[158:159], v[112:113], v[32:47]\n"
      "v_exp_f32 v80, v80\n"
      "v_exp_f32 v81, v81\n"
      "v_mfma_f32_32x32x16_bf16 v[0:15], v[136:139], v[72:75], v[0:15]\n"
      "v_exp_f32 v82, v82\n"
      "s_sub_u32 s46, s46, 1\n"
      "v_exp_f32 v83, v83\n"
      "s_cbranch_scc0 Lnpf_%=\n"
      "s_setprio 0\n"
      "Lnpf_%=:\n"
      "v_exp_f32 v84, v84\n"
      "v_exp_f32 v85, v85\n"
      "v_mfma_f32_32x32x16_bf16 v[64:79], v[120:123], v[108:111], 0\n"
      "v_exp_f32 v86, v86\n"
      "v_exp_f32 v87, v87\n"
      "v_cvt_pk_bf16_f32 v80, v80, v81\n"
      "v_cvt_pk_bf16_f32 v81, v82, v83\n"
      "v_max3_f32 v32, v32, v33, v34\n"
      "v_max3_f32 v35, v35, v36, v37\n"
      "v_max3_f32 v32, v32, v35, v38\n"
      "s_mov_b64 exec, s[62:63]\n"
      "v_cvt_pk_bf16_f32 v112, -v32, 0\n"
      "s_mov_b64 exec, -1\n"
      "v_cvt_pk_bf16_f32 v82, v84, v85\n"
      "v_cvt_pk_bf16_f32 v83, v86, v87\n"
      "v_mfma_f32_32x32x8bf16_1k v[64:79], v[124:125], v[112:113], v[64:79]\n"
      "v_exp_f32 v88, v88\n"
      "v_exp_f32 v89, v89\n"
      "v_exp_f32 v90, v90\n"
      "v_exp_f32 v91, v91\n"
      "v_exp_f32 v92, v92\n"
      "v_mfma_f32_32x32x16_bf16 v[16:31], v[132:135], v[80:83], 0\n"
      "v_exp_f32 v93, v93\n"
      "v_exp_f32 v94, v94\n"
      "v_exp_f32 v95, v95\n"
      "v_cvt_pk_bf16_f32 v88, v88, v89\n"
      "v_mfma_f32_32x32x16_bf16 v[48:63], v[154:157], v[114:117], 0\n"
      "v_cvt_pk_bf16_f32 v89, v90, v91\n"
      "v_cvt_pk_bf16_f32 v90, v92, v93\n"
      "v_cvt_pk_bf16_f32 v91, v94, v95\n"
      "v_mfma_f32_32x32x8bf16_1k v[48:63], v[158:159], v[118:119], v[48:63]\n"
      "v_exp_f32 v64, v64\n"
      "v_exp_f32 v65, v65\n"
      "v_mfma_f32_32x32x16_bf16 v[16:31], v[136:139], v[88:91], v[16:31]\n"
      "v_exp_f32 v66, v66\n"
      "v_exp_f32 v67, v67\n"
      "v_exp_f32 v68, v68\n"
      "v_exp_f32 v69, v69\n"
      "v_mfma_f32_32x32x16_bf16 v[80:95], v[120:123], v[114:117], 0\n"
      "v_exp_f32 v70, v70\n"
      "v_exp_f32 v71, v71\n"
      "v_cvt_pk_bf16_f32 v64, v64, v65\n"
      "v_cvt_pk_bf16_f32 v65, v66, v67\n"
      "v_max3_f32 v48, v48, v49, v50\n"
      "v_max3_f32 v51, v51, v52, v53\n"
      "v_max3_f32 v48, v48, v51, v54\n"
      "s_mov_b64 exec, s[62:63]\n"
      "v_cvt_pk_bf16_f32 v118, -v48, 0\n"
      "s_mov_b64 exec, -1\n"
      "v_cvt_pk_bf16_f32 v66, v68, v69\n"
      "v_cvt_pk_bf16_f32 v67, v70, v71\n"
      "v_mfma_f32_32x32x8bf16_1k v[80:95], v[124:125], v[118:119], v[80:95]\n"
      "v_exp_f32 v72, v72\n"
      "v_exp_f32 v73, v73\n"
      "v_exp_f32 v74, v74\n"
      "v_exp_f32 v75, v75\n"
      "v_exp_f32 v76, v76\n"
      "v_mfma_f32_32x32x16_bf16 v[32:47], v[132:135], v[64:67], 0\n"
      "v_exp_f32 v77, v77\n"
      "v_exp_f32 v78, v78\n"
      "v_exp_f32 v79, v79\n"
      "v_cvt_pk_bf16_f32 v72, v72, v73\n"
      "v_cvt_pk_bf16_f32 v73, v74, v75\n"
      "v_cvt_pk_bf16_f32 v74, v76, v77\n"
      "v_cvt_pk_bf16_f32 v75, v78, v79\n"
      "v_exp_f32 v80, v80\n"
      "buffer_load_dwordx4 v[120:123], v148, s[40:43], s44 offen sc1\n"
      "v_exp_f32 v81, v81\n"
      "s_mov_b64 exec, s[50:51]\n"
      "buffer_load_dwordx2 v[124:125], v149, s[40:43], s44 offen sc1\n"
      "s_mov_b64 exec, -1\n"
      "v_mfma_f32_32x32x16_bf16 v[32:47], v[136:139], v[72:75], v[32:47]\n"
      "v_exp_f32 v82, v82\n"
      "v_exp_f32 v83, v83\n"
      "v_exp_f32 v84, v84\n"
      "v_exp_f32 v85, v85\n"
      "s_waitcnt vmcnt(4)\n"
      "v_mfma_f32_32x32x16_bf16 v[64:79], v[126:129], v[96:99], 0\n"
      "v_exp_f32 v86, v86\n"
      "v_exp_f32 v87, v87\n"
      "v_cvt_pk_bf16_f32 v80, v80, v81\n"
      "v_cvt_pk_bf16_f32 v81, v82, v83\n"
      "v_cvt_pk_bf16_f32 v82, v84, v85\n"
      "v_cvt_pk_bf16_f32 v83, v86, v87\n"
      "v_mfma_f32_32x32x8bf16_1k v[64:79], v[130:131], v[100:101], v[64:79]\n"
      "v_exp_f32 v88, v88\n"
      "v_exp_f32 v89, v89\n"
      "v_exp_f32 v90, v90\n"
      "v_exp_f32 v91, v91\n"
      "v_exp_f32 v92, v92\n"
      "v_mfma_f32_32x32x16_bf16 v[48:63], v[132:135], v[80:83], 0\n"
      "v_exp_f32 v93, v93\n"
      "v_exp_f32 v94, v94\n"
      "v_exp_f32 v95, v95\n"
      "v_cvt_pk_bf16_f32 v88, v88, v89\n"
      "v_cvt_pk_bf16_f32 v89, v90, v91\n"
      "v_cvt_pk_bf16_f32 v90, v92, v93\n"
      "v_cvt_pk_bf16_f32 v91, v94, v95\n"
      "s_branch Lmid_%=\n"
      "Lloop_%=:\n"
      "v_exp_f32 v64, v64\n"
      "v_exp_f32 v65, v65\n"
      "v_mfma_f32_32x32x16_bf16 v[48:63], v[144:147], v[88:91], v[48:63]\n"
      "v_exp_f32 v66, v66\n"
      "s_mov_b64 exec, s[52:53]\n"
      "buffer_load_dwordx4 v[140:143], v150, s[40:43], s44 offen sc1\n"
      "buffer_load_dwordx4 v[144:147], v150, s[40:43], s44 offen offset:640 sc1\n"
      "s_mov_b64 exec, -1\n"
      "v_exp_f32 v67, v67\n"
      "s_add_u32 s44, s44, 20480\n"
      "v_exp_f32 v68, v68\n"
      "s_cmp_ge_u32 s44, s45\n"
      "v_exp_f32 v69, v69\n"
      "s_cselect_b32 s48, 327680, 0\n"
      "v_mfma_f32_32x32x16_bf16 v[80:95], v[120:123], v[102:105], 0\n"
      "v_exp_f32 v70, v70\n"
      "s_sub_u32 s44, s44, s48\n"
      "v_exp_f32 v71, v71\n"
      "v_cvt_pk_bf16_f32 v64, v64, v65\n"
      "v_cvt_pk_bf16_f32 v65, v66, v67\n"
      "v_cvt_pk_bf16_f32 v66, v68, v69\n"
      "v_cvt_pk_bf16_f32 v67, v70, v71\n"
      "v_mfma_f32_32x32x8bf16_1k v[80:95], v[124:125], v[106:107], v[80:95]\n"
      "v_exp_f32 v72, v72\n"
      "v_exp_f32 v73, v73\n"
      "v_exp_f32 v74, v74\n"
      "v_exp_f32 v75, v75\n"
      "v_exp_f32 v76, v76\n"
      "s_waitcnt vmcnt(4)\n"
      "v_mfma_f32_32x32x16_bf16 v[0:15], v[132:135], v[64:67], v[0:15]\n"
      "v_exp_f32 v77, v77\n"
      "v_exp_f32 v78, v78\n"
      "v_exp_f32 v79, v79\n"
      "v_cvt_pk_bf16_f32 v72, v72, v73\n"
      "v_cvt_pk_bf16_f32 v73, v74, v75\n"
      "v_cvt_pk_bf16_f32 v74, v76, v77\n"
      "v_cvt_pk_bf16_f32 v75, v78, v79\n"
      "v_exp_f32 v80, v80\n"
      "v_exp_f32 v81, v81\n"
      "v_mfma_f32_32x32x16_bf16 v[0:15], v[136:139], v[72:75], v[0:15]\n"
      "v_exp_f32 v82, v82\n"
      "s_sub_u32 s46, s46, 1\n"
      "v_exp_f32 v83, v83\n"
      "s_cbranch_scc0 Lnp0_%=\n"
      "s_setprio 0\n"
      "Lnp0_%=:\n"
      "v_exp_f32 v84, v84\n"
      "v_exp_f32 v85, v85\n"
      "v_mfma_f32_32x32x16_bf16 v[64:79], v[120:123], v[108:111], 0\n"
      "v_exp_f32 v86, v86\n"
      "v_exp_f32 v87, v87\n"
      "v_cvt_pk_bf16_f32 v80, v80, v81\n"
      "v_cvt_pk_bf16_f32 v81, v82, v83\n"
      "v_cvt_pk_bf16_f32 v82, v84, v85\n"
      "v_cvt_pk_bf16_f32 v83, v86, v87\n"
      "v_mfma_f32_32x32x8bf16_1k v[64:79], v[124:125], v[112:113], v[64:79]\n"
      "v_exp_f32 v88, v88\n"
      "v_exp_f32 v89, v89\n"
      "v_exp_f32 v90, v90\n"
      "v_exp_f32 v91, v91\n"
      "v_exp_f32 v92, v92\n"
      "v_mfma_f32_32x32x16_bf16 v[16:31], v[132:135], v[80:83], v[16:31]\n"
      "v_exp_f32 v93, v93\n"
      "v_exp_f32 v94, v94\n"
      "v_exp_f32 v95, v95\n"
      "v_cvt_pk_bf16_f32 v88, v88, v89\n"
      "v_cvt_pk_bf16_f32 v89, v90, v91\n"
      "v_cvt_pk_bf16_f32 v90, v92, v93\n"
      "v_cvt_pk_bf16_f32 v91, v94, v95\n"
      "v_exp_f32 v64, v64\n"
      "v_exp_f32 v65, v65\n"
      "v_mfma_f32_32x32x16_bf16 v[16:31], v[136:139], v[88:91], v[16:31]\n"
      "v_exp_f32 v66, v66\n"
      "v_exp_f32 v67, v67\n"
      "v_exp_f32 v68, v68\n"
      "v_exp_f32 v69, v69\n"
      "v_mfma_f32_32x32x16_bf16 v[80:95], v[120:123], v[114:117], 0\n"
      "v_exp_f32 v70, v70\n"
      "v_exp_f32 v71, v71\n"
      "v_cvt_pk_bf16_f32 v64, v64, v65\n"
      "v_cvt_pk_bf16_f32 v65, v66, v67\n"
      "v_cvt_pk_bf16_f32 v66, v68, v69\n"
      "v_cvt_pk_bf16_f32 v67, v70, v71\n"
      "v_mfma_f32_32x32x8bf16_1k v[80:95], v[124:125], v[118:119], v[80:95]\n"
      "v_exp_f32 v72, v72\n"
      "v_exp_f32 v73, v73\n"
      "v_exp_f32 v74, v74\n"
      "v_exp_f32 v75, v75\n"
      "v_exp_f32 v76, v76\n"
      "v_mfma_f32_32x32x16_bf16 v[32:47], v[132:135], v[64:67], v[32:47]\n"
      "v_exp_f32 v77, v77\n"
      "v_exp_f32 v78, v78\n"
      "v_exp_f32 v79, v79\n"
      "v_cvt_pk_bf16_f32 v72, v72, v73\n"
      "v_cvt_pk_bf16_f32 v73, v74, v75\n"
      "v_cvt_pk_bf16_f32 v74, v76, v77\n"
      "v_cvt_pk_bf16_f32 v75, v78, v79\n"
      "v_exp_f32 v80, v80\n"
      "buffer_load_dwordx4 v[120:123], v148, s[40:43], s44 offen sc1\n"
      "v_exp_f32 v81, v81\n"
      "s_mov_b64 exec, s[50:51]\n"
      "buffer_load_dwordx2 v[124:125], v149, s[40:43], s44 offen sc1\n"
      "s_mov_b64 exec, -1\n"
      "v_mfma_f32_32x32x16_bf16 v[32:47], v[136:139], v[72:75], v[32:47]\n"
      "v_exp_f32 v82, v82\n"
      "v_exp_f32 v83, v83\n"
      "v_exp_f32 v84, v84\n"
      "v_exp_f32 v85, v85\n"
      "s_waitcnt vmcnt(4)\n"
      "v_mfma_f32_32x32x16_bf16 v[64:79], v[126:129], v[96:99], 0\n"
      "v_exp_f32 v86, v86\n"
      "v_exp_f32 v87, v87\n"
      "v_cvt_pk_bf16_f32 v80, v80, v81\n"
      "v_cvt_pk_bf16_f32 v81, v82, v83\n"
      "v_cvt_pk_bf16_f32 v82, v84, v85\n"
      "v_cvt_pk_bf16_f32 v83, v86, v87\n"
      "v_mfma_f32_32x32x8bf16_1k v[64:79], v[130:131], v[100:101], v[64:79]\n"
      "v_exp_f32 v88, v88\n"
      "v_exp_f32 v89, v89\n"
      "v_exp_f32 v90, v90\n"
      "v_exp_f32 v91, v91\n"
      "v_exp_f32 v92, v92\n"
      "v_mfma_f32_32x32x16_bf16 v[48:63], v[132:135], v[80:83], v[48:63]\n"
      "v_exp_f32 v93, v93\n"
      "v_exp_f32 v94, v94\n"
      "v_exp_f32 v95, v95\n"
      "v_cvt_pk_bf16_f32 v88, v88, v89\n"
      "v_cvt_pk_bf16_f32 v89, v90, v91\n"
      "v_cvt_pk_bf16_f32 v90, v92, v93\n"
      "v_cvt_pk_bf16_f32 v91, v94, v95\n"
      "Lmid_%=:\n"
      "v_exp_f32 v64, v64\n"
      "v_exp_f32 v65, v65\n"
      "v_mfma_f32_32x32x16_bf16 v[48:63], v[136:139], v[88:91], v[48:63]\n"
      "v_exp_f32 v66, v66\n"
      "s_mov_b64 exec, s[52:53]\n"
      "buffer_load_dwordx4 v[132:135], v150, s[40:43], s44 offen sc1\n"
      "buffer_load_dwordx4 v[136:139], v150, s[40:43], s44 offen offset:640 sc1\n"
      "s_mov_b64 exec, -1\n"
      "v_exp_f32 v67, v67\n"
      "s_add_u32 s44, s44, 20480\n"
      "v_exp_f32 v68, v68\n"
      "s_cmp_ge_u32 s44, s45\n"
      "v_exp_f32 v69, v69\n"
      "s_cselect_b32 s48, 327680, 0\n"
      "v_mfma_f32_32x32x16_bf16 v[80:95], v[126:129], v[102:105], 0\n"
      "v_exp_f32 v70, v70\n"
      "s_sub_u32 s44, s44, s48\n"
      "v_exp_f32 v71, v71\n"
      "v_cvt_pk_bf16_f32 v64, v64, v65\n"
      "v_cvt_pk_bf16_f32 v65, v66, v67\n"
      "v_cvt_pk_bf16_f32 v66, v68, v69\n"
      "v_cvt_pk_bf16_f32 v67, v70, v71\n"
      "v_mfma_f32_32x32x8bf16_1k v[80:95], v[130:131], v[106:107], v[80:95]\n"
      "v_exp_f32 v72, v72\n"
      "v_exp_f32 v73, v73\n"
      "v_exp_f32 v74, v74\n"
      "v_exp_f32 v75, v75\n"
      "v_exp_f32 v76, v76\n"
      "s_waitcnt vmcnt(4)\n"
      "v_mfma_f32_32x32x16_bf16 v[0:15], v[140:143], v[64:67], v[0:15]\n"
      "v_exp_f32 v77, v77\n"
      "v_exp_f32 v78, v78\n"
      "v_exp_f32 v79, v79\n"
      "v_cvt_pk_bf16_f32 v72, v72, v73\n"
      "v_cvt_pk_bf16_f32 v73, v74, v75\n"
      "v_cvt_pk_bf16_f32 v74, v76, v77\n"
      "v_cvt_pk_bf16_f32 v75, v78, v79\n"
      "v_exp_f32 v80, v80\n"
      "v_exp_f32 v81, v81\n"
      "v_mfma_f32_32x32x16_bf16 v[0:15], v[144:147], v[72:75], v[0:15]\n"
      "v_exp_f32 v82, v82\n"
      "s_sub_u32 s46, s46, 1\n"
      "v_exp_f32 v83, v83\n"
      "s_cbranch_scc0 Lnp1_%=\n"
      "s_setprio 0\n"
      "Lnp1_%=:\n"
      "v_exp_f32 v84, v84\n"
      "v_exp_f32 v85, v85\n"
      "v_mfma_f32_32x32x16_bf16 v[64:79], v[126:129], v[108:111], 0\n"
      "v_exp_f32 v86, v86\n"
      "v_exp_f32 v87, v87\n"
      "v_cvt_pk_bf16_f32 v80, v80, v81\n"
      "v_cvt_pk_bf16_f32 v81, v82, v83\n"
      "v_cvt_pk_bf16_f32 v82, v84, v85\n"
      "v_cvt_pk_bf16_f32 v83, v86, v87\n"
      "v_mfma_f32_32x32x8bf16_1k v[64:79], v[130:131], v[112:113], v[64:79]\n"
      "v_exp_f32 v88, v88\n"
      "v_exp_f32 v89, v89\n"
      "v_exp_f32 v90, v90\n"
      "v_exp_f32 v91, v91\n"
      "v_exp_f32 v92, v92\n"
      "v_mfma_f32_32x32x16_bf16 v[16:31], v[140:143], v[80:83], v[16:31]\n"
      "v_exp_f32 v93, v93\n"
      "v_exp_f32 v94, v94\n"
      "v_exp_f32 v95, v95\n"
      "v_cvt_pk_bf16_f32 v88, v88, v89\n"
      "v_cvt_pk_bf16_f32 v89, v90, v91\n"
      "v_cvt_pk_bf16_f32 v90, v92, v93\n"
      "v_cvt_pk_bf16_f32 v91, v94, v95\n"
      "v_exp_f32 v64, v64\n"
      "v_exp_f32 v65, v65\n"
      "v_mfma_f32_32x32x16_bf16 v[16:31], v[144:147], v[88:91], v[16:31]\n"
      "v_exp_f32 v66, v66\n"
      "v_exp_f32 v67, v67\n"
      "v_exp_f32 v68, v68\n"
      "v_exp_f32 v69, v69\n"
      "v_mfma_f32_32x32x16_bf16 v[80:95], v[126:129], v[114:117], 0\n"
      "v_exp_f32 v70, v70\n"
      "v_exp_f32 v71, v71\n"
      "v_cvt_pk_bf16_f32 v64, v64, v65\n"
      "v_cvt_pk_bf16_f32 v65, v66, v67\n"
      "v_cvt_pk_bf16_f32 v66, v68, v69\n"
      "v_cvt_pk_bf16_f32 v67, v70, v71\n"
      "v_mfma_f32_32x32x8bf16_1k v[80:95], v[130:131], v[118:119], v[80:95]\n"
      "v_exp_f32 v72, v72\n"
      "v_exp_f32 v73, v73\n"
      "v_exp_f32 v74, v74\n"
      "v_exp_f32 v75, v75\n"
      "v_exp_f32 v76, v76\n"
      "v_mfma_f32_32x32x16_bf16 v[32:47], v[140:143], v[64:67], v[32:47]\n"
      "v_exp_f32 v77, v77\n"
      "v_exp_f32 v78, v78\n"
      "v_exp_f32 v79, v79\n"
      "v_cvt_pk_bf16_f32 v72, v72, v73\n"
      "v_cvt_pk_bf16_f32 v73, v74, v75\n"
      "v_cvt_pk_bf16_f32 v74, v76, v77\n"
      "v_cvt_pk_bf16_f32 v75, v78, v79\n"
      "v_exp_f32 v80, v80\n"
      "buffer_load_dwordx4 v[126:129], v148, s[40:43], s44 offen sc1\n"
      "v_exp_f32 v81, v81\n"
      "s_mov_b64 exec, s[50:51]\n"
      "buffer_load_dwordx2 v[130:131], v149, s[40:43], s44 offen sc1\n"
      "s_mov_b64 exec, -1\n"
      "v_mfma_f32_32x32x16_bf16 v[32:47], v[144:147], v[72:75], v[32:47]\n"
      "v_exp_f32 v82, v82\n"
      "s_sub_u32 s47, s47, 1\n"
      "v_exp_f32 v83, v83\n"
      "s_cmp_lg_u32 s47, 0\n"
      "v_exp_f32 v84, v84\n"
      "v_exp_f32 v85, v85\n"
      "s_waitcnt vmcnt(4)\n"
      "v_mfma_f32_32x32x16_bf16 v[64:79], v[120:123], v[96:99], 0\n"
      "v_exp_f32 v86, v86\n"
      "v_exp_f32 v87, v87\n"
      "v_cvt_pk_bf16_f32 v80, v80, v81\n"
      "v_cvt_pk_bf16_f32 v81, v82, v83\n"
      "v_cvt_pk_bf16_f32 v82, v84, v85\n"
      "v_cvt_pk_bf16_f32 v83, v86, v87\n"
      "v_mfma_f32_32x32x8bf16_1k v[64:79], v[124:125], v[100:101], v[64:79]\n"
      "v_exp_f32 v88, v88\n"
      "v_exp_f32 v89, v89\n"
      "v_exp_f32 v90, v90\n"
      "v_exp_f32 v91, v91\n"
      "v_exp_f32 v92, v92\n"
      "v_mfma_f32_32x32x16_bf16 v[48:63], v[140:143], v[80:83], v[48:63]\n"
      "v_exp_f32 v93, v93\n"
      "v_exp_f32 v94, v94\n"
      "v_exp_f32 v95, v95\n"
      "v_cvt_pk_bf16_f32 v88, v88, v89\n"
      "v_cvt_pk_bf16_f32 v89, v90, v91\n"
      "v_cvt_pk_bf16_f32 v90, v92, v93\n"
      "v_cvt_pk_bf16_f32 v91, v94, v95\n"
      "s_cbranch_scc1 Lloop_%=\n"
      "s_nop 1\n"
      "v_mfma_f32_32x32x16_bf16 v[48:63], v[144:147], v[88:91], v[48:63]\n"
      "s_waitcnt vmcnt(0)\n"
      "s_nop 7\n"
      "s_nop 7\n"
        : "={v[0:15]}"(o0), "={v[16:31]}"(o1), "={v[32:47]}"(o2), "={v[48:63]}"(o3), "+{s44}"(soff), "+{s46}"(sw), "+{s47}"(iters),
          "+{v[96:99]}"(q16[0]), "+{v[100:101]}"(q8[0]), "+{v[102:105]}"(q16[1]), "+{v[106:107]}"(q8[1]), "+{v[108:111]}"(q16[2]), "+{v[112:113]}"(q8[2]),
          "+{v[114:117]}"(q16[3]), "+{v[118:119]}"(q8[3]), "+{v148}"(vk16), "+{v149}"(vk8), "+{v150}"(vv)
        : "{v151}"(rdy), "{v152}"(pre), "{s45}"(send), "{s[40:43]}"(srd), "{s54}"(attempt), "{s55}"(kv0 + t0 * PT), "{s56}"(qo), "{s57}"(qo + 5120)
        : "v64","v65","v66","v67","v68","v69","v70","v71","v72","v73","v74","v75","v76","v77","v78","v79","v80","v81","v82","v83","v84","v85","v86","v87","v88","v89","v90","v91","v92","v93","v94","v95","v120","v121","v122","v123","v124","v125","v126","v127","v128","v129","v130","v131","v132","v133","v134","v135","v136","v137","v138","v139","v140","v141","v142","v143","v144","v145","v146","v147","v154","v155","v156","v157","v158","v159", "s48", "s49", "s50", "s51", "s52", "s53", "s62", "s63", "scc", "memory");
    }
    const int lane = fresh_lane(), r = lane & 31, h = lane >> 5;
    const unsigned cb = L_CMB + (w & 3) * 12288 + lane * 16;
    auto put = [&](const f32x16& o, unsigned ad) {
#pragma unroll
      for (int k = 0; k < 3; ++k) lds_st<f32x4>(ad + k * 1024, f32x4{o[4 * k], o[4 * k + 1], o[4 * k + 2], o[4 * k + 3]}); };
    auto add2 = [&](f32x16& p0, f32x16& p1, unsigned ad) {
      f32x4 t[6];
#pragma unroll
      for (int k = 0; k < 6; ++k) t[k] = lds_ld<f32x4>(ad + k * 1024);
      __builtin_amdgcn_sched_barrier(0);
#pragma unroll
      for (int k = 0; k < 12; ++k) { p0[k] += t[k >> 2][k & 3]; p1[k] += t[3 + (k >> 2)][k & 3]; }
      asm volatile("" : "+v"(p0), "+v"(p1)); };
    auto signal = [&]() { asm volatile("s_waitcnt lgkmcnt(0)" ::: "memory"); if (lane == 0) __hip_atomic_store((LAS unsigned*)(uintptr_t)(L_PAIR + w * 4), attempt + 1, RLX_WG); };
    if (w < 4) {
      wait_lds(L_AUX, 1);
      put(o0, cb); put(o1, cb + 3072); put(o2, cb + 6144); put(o3, cb + 9216);
      signal();
    } else {
      __builtin_amdgcn_s_setprio(3);
      bool bad = false;
      unsigned ob0 = WS_O + ((rb * 8 + (w & 2) * 2 + (r >> 4)) * 80 + 5 * hp) * 256 + (r & 15) * 16, ob1 = h ? 0x80000000u : ob0 + 512 + a * 8;
      ob0 += (3 * a + h) * 256;
      asm volatile("" : "+v"(ob0), "+v"(ob1));
      auto norm = [&](const f32x16& o, int qb) {
        const auto t = __builtin_amdgcn_permlane32_swap(__float_as_uint(o[8]), __float_as_uint(o[8]), false, false);
        const float l = __uint_as_float(t[1]), inv = __builtin_amdgcn_rcpf(l);
        const u32x4 pa = {pk2(o[0] * inv, o[1] * inv), pk2(o[2] * inv, o[3] * inv), pk2(o[4] * inv, o[5] * inv), pk2(o[6] * inv, o[7] * inv)};
        const u32x2 pr = {pk2(o[8] * inv, o[9] * inv), pk2(o[10] * inv, o[11] * inv)};
        __builtin_amdgcn_raw_buffer_store_b128(pa, rs_ws, ob0 + (qb & 1) * 40960, 0, SC1);
        __builtin_amdgcn_raw_buffer_store_b64(pr, rs_ws, ob1 + (qb & 1) * 40960, 0, SC1);
        const float chk = ((o[0] + o[1]) + (o[2] + o[3])) + ((o[4] + o[5]) + (o[6] + o[7])) + ((o[8] + o[9]) + (o[10] + o[11]));
        bad |= !(l < 0x1p64f && l > 0x1p-64f) || !(__builtin_fabsf(chk) < __builtin_inff()); };
      auto merge = [&](f32x16& k0, f32x16& k1, f32x16& g0, f32x16& g1, int qk) {
        const unsigned gv = cb + (2 - qk) * 3072, kp = cb + qk * 3072, pb = L_CMB + ((w ^ 2) & 3) * 12288 + lane * 16 + qk * 3072;
        add2(g0, g1, gv);
        put(g0, gv); put(g1, gv + 3072);
        signal();
        add2(k0, k1, kp);
        wait_lds(L_PAIR + (w ^ 2) * 4, attempt + 1);
        add2(k0, k1, pb);
        norm(k0, qk); norm(k1, qk + 1); };
      wait_lds(L_PAIR + (w - 4) * 4, attempt + 1);
      if (w < 6) merge(o0, o1, o2, o3, 0); else merge(o2, o3, o0, o1, 2);
      if (__any(bad) && lane == 0) lds_st<unsigned>(L_BAD, 1u);
      asm volatile("s_waitcnt lgkmcnt(0)" ::: "memory");
      if (lane == 0) __hip_atomic_fetch_add((LAS unsigned*)(uintptr_t)L_DEC, 1u, RLX_WG);
      asm volatile("s_waitcnt vmcnt(0) lgkmcnt(0)" ::: "memory");
      unsigned tl = tag_lo, th = tag_hi;
      asm volatile("" : "+s"(tl), "+s"(th));
      unsigned old = 0;
      if (lane == 0) old = __hip_atomic_fetch_add((LAS unsigned*)(uintptr_t)L_FIN, 1u, RLX_WG);
      if (uni(old) == 4 * attempt + 3 && (attempt || uni(lds_ld<unsigned>(L_BAD)) == 0) && lane == 0)
        __hip_atomic_store((GAS u64*)(ws + WS_OF + (rb * 16 + hp) * FS), ((u64)th << 32) | tl, RLX_AGENT);
    }
    wait_lds(L_DEC, 4 * attempt + 4);
    if (attempt || uni(lds_ld<unsigned>(L_BAD)) == 0) break;
  }
  {
    const int lane = fresh_lane();
    __builtin_amdgcn_s_setprio(0);
    const int m = w & 3, kh = w >> 2, l15 = lane & 15, g = lane >> 4, q = l15 >> 2, p = lane & 3;
    const unsigned X = ((q >> 1) & 1) | ((g & 1) << 1), ab = L_WOA + kh * 40960 + g * 1024 + q * 128 + 8 * p;
    const unsigned bb0 = L_WOB + kh * 10240 + g * 256 + ((g & 1) << 7) + q * 32 + 8 * p, bb1 = bb0 ^ 128;
    const unsigned oo = WS_O + (rb64 * 4 + m) * 20480 + kh * 10240 + lane * 16;
    const u64 wb = (u64)ws;
    const u32x4 srd = {(unsigned)wb, (unsigned)(wb >> 32), WS_END, 0x00020000u};
    wait_lds(L_WOD, 3);
    unsigned pm = 0;
#pragma unroll 1
    for (unsigned spin = 0; spin < SPIN_CAP; ++spin) {
      pm = uni(__hip_atomic_load((LAS unsigned*)(uintptr_t)L_OMASK, RLX_WG)) >> (kh * 8);
      if ((pm & 15) == 15 || (pm & 240) == 240) break;
      __builtin_amdgcn_s_sleep(1);
    }
    asm volatile("" ::: "memory");
    const unsigned ch = 1 - (((pm & 15) + 1) >> 4);
    const unsigned a0 = ab + ch * 20480;
    u32x4 an = {a0 + (X << 5), a0 + ((1 ^ X) << 5), a0 + ((2 ^ X) << 5), a0 + ((3 ^ X) << 5)};
    unsigned b0 = bb0 + ch * 5120, b1 = bb1 + ch * 5120;
    f32x16 acc, t; f32x4 acc4, t4;
    asm volatile(
        "buffer_load_dwordx4 v[40:43], v126, s[40:43], s44 offen offset:0 sc1\n"
        "buffer_load_dwordx4 v[44:47], v126, s[40:43], s44 offen offset:1024 sc1\n"
        "buffer_load_dwordx4 v[48:51], v126, s[40:43], s44 offen offset:2048 sc1\n"
        "buffer_load_dwordx4 v[52:55], v126, s[40:43], s44 offen offset:3072 sc1\n"
        "buffer_load_dwordx4 v[56:59], v126, s[40:43], s45 offen offset:0 sc1\n"
        "s_cmp_lg_u32 s50, 0\n"
        "s_cbranch_scc1 Lboth_%=\n"
        "ds_read_b64_tr_b16 v[80:81], v120 offset:0\n"
        "ds_read_b64_tr_b16 v[82:83], v120 offset:512\n"
        "ds_read_b64_tr_b16 v[84:85], v121 offset:0\n"
        "ds_read_b64_tr_b16 v[86:87], v121 offset:512\n"
        "ds_read_b64_tr_b16 v[88:89], v122 offset:0\n"
        "ds_read_b64_tr_b16 v[90:91], v122 offset:512\n"
        "ds_read_b64_tr_b16 v[92:93], v123 offset:0\n"
        "ds_read_b64_tr_b16 v[94:95], v123 offset:512\n"
        "ds_read_b64_tr_b16 v[96:97], v124 offset:0\n"
        "ds_read_b64_tr_b16 v[98:99], v125 offset:0\n"
        "ds_read_b64_tr_b16 v[100:101], v120 offset:4096\n"
        "ds_read_b64_tr_b16 v[102:103], v120 offset:4608\n"
        "ds_read_b64_tr_b16 v[104:105], v121 offset:4096\n"
        "ds_read_b64_tr_b16 v[106:107], v121 offset:4608\n"
        "ds_read_b64_tr_b16 v[108:109], v122 offset:4096\n"
        "ds_read_b64_tr_b16 v[110:111], v122 offset:4608\n"
        "ds_read_b64_tr_b16 v[112:113], v123 offset:4096\n"
        "ds_read_b64_tr_b16 v[114:115], v123 offset:4608\n"
        "ds_read_b64_tr_b16 v[116:117], v124 offset:1024\n"
        "ds_read_b64_tr_b16 v[118:119], v125 offset:1024\n"
        "s_waitcnt vmcnt(4) lgkmcnt(10)\n"
        "v_mfma_f32_16x16x32_bf16 v[0:3], v[80:83], v[40:43], 0\n"
        "ds_read_b64_tr_b16 v[80:81], v120 offset:8192\n"
        "ds_read_b64_tr_b16 v[82:83], v120 offset:8704\n"
        "v_mfma_f32_16x16x32_bf16 v[4:7], v[84:87], v[40:43], 0\n"
        "ds_read_b64_tr_b16 v[84:85], v121 offset:8192\n"
        "ds_read_b64_tr_b16 v[86:87], v121 offset:8704\n"
        "v_mfma_f32_16x16x32_bf16 v[8:11], v[88:91], v[40:43], 0\n"
        "ds_read_b64_tr_b16 v[88:89], v122 offset:8192\n"
        "ds_read_b64_tr_b16 v[90:91], v122 offset:8704\n"
        "v_mfma_f32_16x16x32_bf16 v[12:15], v[92:95], v[40:43], 0\n"
        "ds_read_b64_tr_b16 v[92:93], v123 offset:8192\n"
        "ds_read_b64_tr_b16 v[94:95], v123 offset:8704\n"
        "v_mfma_f32_16x16x32_bf16 v[16:19], v[96:99], v[40:43], 0\n"
        "ds_read_b64_tr_b16 v[96:97], v124 offset:2048\n"
        "ds_read_b64_tr_b16 v[98:99], v125 offset:2048\n"
        "s_waitcnt vmcnt(3) lgkmcnt(10)\n"
        "v_mfma_f32_16x16x32_bf16 v[0:3], v[100:103], v[44:47], v[0:3]\n"
        "ds_read_b64_tr_b16 v[100:101], v120 offset:12288\n"
        "ds_read_b64_tr_b16 v[102:103], v120 offset:12800\n"
        "v_mfma_f32_16x16x32_bf16 v[4:7], v[104:107], v[44:47], v[4:7]\n"
        "ds_read_b64_tr_b16 v[104:105], v121 offset:12288\n"
        "ds_read_b64_tr_b16 v[106:107], v121 offset:12800\n"
        "v_mfma_f32_16x16x32_bf16 v[8:11], v[108:111], v[44:47], v[8:11]\n"
        "ds_read_b64_tr_b16 v[108:109], v122 offset:12288\n"
        "ds_read_b64_tr_b16 v[110:111], v122 offset:12800\n"
        "v_mfma_f32_16x16x32_bf16 v[12:15], v[112:115], v[44:47], v[12:15]\n"
        "ds_read_b64_tr_b16 v[112:113], v123 offset:12288\n"
        "ds_read_b64_tr_b16 v[114:115], v123 offset:12800\n"
        "v_mfma_f32_16x16x32_bf16 v[16:19], v[116:119], v[44:47], v[16:19]\n"
        "ds_read_b64_tr_b16 v[116:117], v124 offset:3072\n"
        "ds_read_b64_tr_b16 v[118:119], v125 offset:3072\n"
        "s_waitcnt vmcnt(2) lgkmcnt(10)\n"
        "v_mfma_f32_16x16x32_bf16 v[0:3], v[80:83], v[48:51], v[0:3]\n"
        "ds_read_b64_tr_b16 v[80:81], v120 offset:16384\n"
        "ds_read_b64_tr_b16 v[82:83], v120 offset:16896\n"
        "v_mfma_f32_16x16x32_bf16 v[4:7], v[84:87], v[48:51], v[4:7]\n"
        "ds_read_b64_tr_b16 v[84:85], v121 offset:16384\n"
        "ds_read_b64_tr_b16 v[86:87], v121 offset:16896\n"
        "v_mfma_f32_16x16x32_bf16 v[8:11], v[88:91], v[48:51], v[8:11]\n"
        "ds_read_b64_tr_b16 v[88:89], v122 offset:16384\n"
        "ds_read_b64_tr_b16 v[90:91], v122 offset:16896\n"
        "v_mfma_f32_16x16x32_bf16 v[12:15], v[92:95], v[48:51], v[12:15]\n"
        "ds_read_b64_tr_b16 v[92:93], v123 offset:16384\n"
        "ds_read_b64_tr_b16 v[94:95], v123 offset:16896\n"
        "v_mfma_f32_16x16x32_bf16 v[16:19], v[96:99], v[48:51], v[16:19]\n"
        "ds_read_b64_tr_b16 v[96:97], v124 offset:4096\n"
        "ds_read_b64_tr_b16 v[98:99], v125 offset:4096\n"
        "s_waitcnt vmcnt(1) lgkmcnt(10)\n"
        "v_mfma_f32_16x16x32_bf16 v[0:3], v[100:103], v[52:55], v[0:3]\n"
        "v_mfma_f32_16x16x32_bf16 v[4:7], v[104:107], v[52:55], v[4:7]\n"
        "v_mfma_f32_16x16x32_bf16 v[8:11], v[108:111], v[52:55], v[8:11]\n"
        "v_mfma_f32_16x16x32_bf16 v[12:15], v[112:115], v[52:55], v[12:15]\n"
        "v_mfma_f32_16x16x32_bf16 v[16:19], v[116:119], v[52:55], v[16:19]\n"
        "s_waitcnt vmcnt(0) lgkmcnt(0)\n"
        "v_mfma_f32_16x16x32_bf16 v[0:3], v[80:83], v[56:59], v[0:3]\n"
        "v_mfma_f32_16x16x32_bf16 v[4:7], v[84:87], v[56:59], v[4:7]\n"
        "v_mfma_f32_16x16x32_bf16 v[8:11], v[88:91], v[56:59], v[8:11]\n"
        "v_mfma_f32_16x16x32_bf16 v[12:15], v[92:95], v[56:59], v[12:15]\n"
        "v_mfma_f32_16x16x32_bf16 v[16:19], v[96:99], v[56:59], v[16:19]\n"
        "s_mov_b32 s51, 0x40000\n"
        "Lspin_%=:\n"
        "ds_read_b32 v60, v127\n"
        "s_sub_u32 s51, s51, 1\n"
        "s_waitcnt lgkmcnt(0)\n"
        "v_readfirstlane_b32 s52, v60\n"
        "s_cmp_eq_u32 s51, 0\n"
        "s_cbranch_scc1 Lgo_%=\n"
        "s_and_b32 s52, s52, s49\n"
        "s_cmp_eq_u32 s52, s49\n"
        "s_cbranch_scc1 Lgo_%=\n"
        "s_sleep 1\n"
        "s_branch Lspin_%=\n"
        "Lgo_%=:\n"
        "buffer_load_dwordx4 v[60:63], v126, s[40:43], s46 offen offset:0 sc1\n"
        "buffer_load_dwordx4 v[64:67], v126, s[40:43], s46 offen offset:1024 sc1\n"
        "buffer_load_dwordx4 v[68:71], v126, s[40:43], s46 offen offset:2048 sc1\n"
        "buffer_load_dwordx4 v[72:75], v126, s[40:43], s46 offen offset:3072 sc1\n"
        "buffer_load_dwordx4 v[76:79], v126, s[40:43], s47 offen offset:0 sc1\n"
        "s_branch Lsec_%=\n"
        "Lboth_%=:\n"
        "buffer_load_dwordx4 v[60:63], v126, s[40:43], s46 offen offset:0 sc1\n"
        "buffer_load_dwordx4 v[64:67], v126, s[40:43], s46 offen offset:1024 sc1\n"
        "buffer_load_dwordx4 v[68:71], v126, s[40:43], s46 offen offset:2048 sc1\n"
        "buffer_load_dwordx4 v[72:75], v126, s[40:43], s46 offen offset:3072 sc1\n"
        "buffer_load_dwordx4 v[76:79], v126, s[40:43], s47 offen offset:0 sc1\n"
        "ds_read_b64_tr_b16 v[80:81], v120 offset:0\n"
        "ds_read_b64_tr_b16 v[82:83], v120 offset:512\n"
        "ds_read_b64_tr_b16 v[84:85], v121 offset:0\n"
        "ds_read_b64_tr_b16 v[86:87], v121 offset:512\n"
        "ds_read_b64_tr_b16 v[88:89], v122 offset:0\n"
        "ds_read_b64_tr_b16 v[90:91], v122 offset:512\n"
        "ds_read_b64_tr_b16 v[92:93], v123 offset:0\n"
        "ds_read_b64_tr_b16 v[94:95], v123 offset:512\n"
        "ds_read_b64_tr_b16 v[96:97], v124 offset:0\n"
        "ds_read_b64_tr_b16 v[98:99], v125 offset:0\n"
        "ds_read_b64_tr_b16 v[100:101], v120 offset:4096\n"
        "ds_read_b64_tr_b16 v[102:103], v120 offset:4608\n"
        "ds_read_b64_tr_b16 v[104:105], v121 offset:4096\n"
        "ds_read_b64_tr_b16 v[106:107], v121 offset:4608\n"
        "ds_read_b64_tr_b16 v[108:109], v122 offset:4096\n"
        "ds_read_b64_tr_b16 v[110:111], v122 offset:4608\n"
        "ds_read_b64_tr_b16 v[112:113], v123 offset:4096\n"
        "ds_read_b64_tr_b16 v[114:115], v123 offset:4608\n"
        "ds_read_b64_tr_b16 v[116:117], v124 offset:1024\n"
        "ds_read_b64_tr_b16 v[118:119], v125 offset:1024\n"
        "s_waitcnt vmcnt(9) lgkmcnt(10)\n"
        "v_mfma_f32_16x16x32_bf16 v[0:3], v[80:83], v[40:43], 0\n"
        "ds_read_b64_tr_b16 v[80:81], v120 offset:8192\n"
        "ds_read_b64_tr_b16 v[82:83], v120 offset:8704\n"
        "v_mfma_f32_16x16x32_bf16 v[4:7], v[84:87], v[40:43], 0\n"
        "ds_read_b64_tr_b16 v[84:85], v121 offset:8192\n"
        "ds_read_b64_tr_b16 v[86:87], v121 offset:8704\n"
        "v_mfma_f32_16x16x32_bf16 v[8:11], v[88:91], v[40:43], 0\n"
        "ds_read_b64_tr_b16 v[88:89], v122 offset:8192\n"
        "ds_read_b64_tr_b16 v[90:91], v122 offset:8704\n"
        "v_mfma_f32_16x16x32_bf16 v[12:15], v[92:95], v[40:43], 0\n"
        "ds_read_b64_tr_b16 v[92:93], v123 offset:8192\n"
        "ds_read_b64_tr_b16 v[94:95], v123 offset:8704\n"
        "v_mfma_f32_16x16x32_bf16 v[16:19], v[96:99], v[40:43], 0\n"
        "ds_read_b64_tr_b16 v[96:97], v124 offset:2048\n"
        "ds_read_b64_tr_b16 v[98:99], v125 offset:2048\n"
        "s_waitcnt vmcnt(8) lgkmcnt(10)\n"
        "v_mfma_f32_16x16x32_bf16 v[0:3], v[100:103], v[44:47], v[0:3]\n"
        "ds_read_b64_tr_b16 v[100:101], v120 offset:12288\n"
        "ds_read_b64_tr_b16 v[102:103], v120 offset:12800\n"
        "v_mfma_f32_16x16x32_bf16 v[4:7], v[104:107], v[44:47], v[4:7]\n"
        "ds_read_b64_tr_b16 v[104:105], v121 offset:12288\n"
        "ds_read_b64_tr_b16 v[106:107], v121 offset:12800\n"
        "v_mfma_f32_16x16x32_bf16 v[8:11], v[108:111], v[44:47], v[8:11]\n"
        "ds_read_b64_tr_b16 v[108:109], v122 offset:12288\n"
        "ds_read_b64_tr_b16 v[110:111], v122 offset:12800\n"
        "v_mfma_f32_16x16x32_bf16 v[12:15], v[112:115], v[44:47], v[12:15]\n"
        "ds_read_b64_tr_b16 v[112:113], v123 offset:12288\n"
        "ds_read_b64_tr_b16 v[114:115], v123 offset:12800\n"
        "v_mfma_f32_16x16x32_bf16 v[16:19], v[116:119], v[44:47], v[16:19]\n"
        "ds_read_b64_tr_b16 v[116:117], v124 offset:3072\n"
        "ds_read_b64_tr_b16 v[118:119], v125 offset:3072\n"
        "s_waitcnt vmcnt(7) lgkmcnt(10)\n"
        "v_mfma_f32_16x16x32_bf16 v[0:3], v[80:83], v[48:51], v[0:3]\n"
        "ds_read_b64_tr_b16 v[80:81], v120 offset:16384\n"
        "ds_read_b64_tr_b16 v[82:83], v120 offset:16896\n"
        "v_mfma_f32_16x16x32_bf16 v[4:7], v[84:87], v[48:51], v[4:7]\n"
        "ds_read_b64_tr_b16 v[84:85], v121 offset:16384\n"
        "ds_read_b64_tr_b16 v[86:87], v121 offset:16896\n"
        "v_mfma_f32_16x16x32_bf16 v[8:11], v[88:91], v[48:51], v[8:11]\n"
        "ds_read_b64_tr_b16 v[88:89], v122 offset:16384\n"
        "ds_read_b64_tr_b16 v[90:91], v122 offset:16896\n"
        "v_mfma_f32_16x16x32_bf16 v[12:15], v[92:95], v[48:51], v[12:15]\n"
        "ds_read_b64_tr_b16 v[92:93], v123 offset:16384\n"
        "ds_read_b64_tr_b16 v[94:95], v123 offset:16896\n"
        "v_mfma_f32_16x16x32_bf16 v[16:19], v[96:99], v[48:51], v[16:19]\n"
        "ds_read_b64_tr_b16 v[96:97], v124 offset:4096\n"
        "ds_read_b64_tr_b16 v[98:99], v125 offset:4096\n"
        "s_waitcnt vmcnt(6) lgkmcnt(10)\n"
        "v_mfma_f32_16x16x32_bf16 v[0:3], v[100:103], v[52:55], v[0:3]\n"
        "v_mfma_f32_16x16x32_bf16 v[4:7], v[104:107], v[52:55], v[4:7]\n"
        "v_mfma_f32_16x16x32_bf16 v[8:11], v[108:111], v[52:55], v[8:11]\n"
        "v_mfma_f32_16x16x32_bf16 v[12:15], v[112:115], v[52:55], v[12:15]\n"
        "v_mfma_f32_16x16x32_bf16 v[16:19], v[116:119], v[52:55], v[16:19]\n"
        "s_waitcnt vmcnt(5) lgkmcnt(0)\n"
        "v_mfma_f32_16x16x32_bf16 v[0:3], v[80:83], v[56:59], v[0:3]\n"
        "v_mfma_f32_16x16x32_bf16 v[4:7], v[84:87], v[56:59], v[4:7]\n"
        "v_mfma_f32_16x16x32_bf16 v[8:11], v[88:91], v[56:59], v[8:11]\n"
        "v_mfma_f32_16x16x32_bf16 v[12:15], v[92:95], v[56:59], v[12:15]\n"
        "v_mfma_f32_16x16x32_bf16 v[16:19], v[96:99], v[56:59], v[16:19]\n"
        "Lsec_%=:\n"
        "s_ashr_i32 s52, s48, 2\n"
        "v_add_u32 v120, s48, v120\n"
        "v_add_u32 v121, s48, v121\n"
        "v_add_u32 v122, s48, v122\n"
        "v_add_u32 v123, s48, v123\n"
        "v_add_u32 v124, s52, v124\n"
        "v_add_u32 v125, s52, v125\n"
        "ds_read_b64_tr_b16 v[80:81], v120 offset:0\n"
        "ds_read_b64_tr_b16 v[82:83], v120 offset:512\n"
        "ds_read_b64_tr_b16 v[84:85], v121 offset:0\n"
        "ds_read_b64_tr_b16 v[86:87], v121 offset:512\n"
        "ds_read_b64_tr_b16 v[88:89], v122 offset:0\n"
        "ds_read_b64_tr_b16 v[90:91], v122 offset:512\n"
        "ds_read_b64_tr_b16 v[92:93], v123 offset:0\n"
        "ds_read_b64_tr_b16 v[94:95], v123 offset:512\n"
        "ds_read_b64_tr_b16 v[96:97], v124 offset:0\n"
        "ds_read_b64_tr_b16 v[98:99], v125 offset:0\n"
        "ds_read_b64_tr_b16 v[100:101], v120 offset:4096\n"
        "ds_read_b64_tr_b16 v[102:103], v120 offset:4608\n"
        "ds_read_b64_tr_b16 v[104:105], v121 offset:4096\n"
        "ds_read_b64_tr_b16 v[106:107], v121 offset:4608\n"
        "ds_read_b64_tr_b16 v[108:109], v122 offset:4096\n"
        "ds_read_b64_tr_b16 v[110:111], v122 offset:4608\n"
        "ds_read_b64_tr_b16 v[112:113], v123 offset:4096\n"
        "ds_read_b64_tr_b16 v[114:115], v123 offset:4608\n"
        "ds_read_b64_tr_b16 v[116:117], v124 offset:1024\n"
        "ds_read_b64_tr_b16 v[118:119], v125 offset:1024\n"
        "s_waitcnt vmcnt(4) lgkmcnt(10)\n"
        "v_mfma_f32_16x16x32_bf16 v[20:23], v[80:83], v[60:63], 0\n"
        "ds_read_b64_tr_b16 v[80:81], v120 offset:8192\n"
        "ds_read_b64_tr_b16 v[82:83], v120 offset:8704\n"
        "v_mfma_f32_16x16x32_bf16 v[24:27], v[84:87], v[60:63], 0\n"
        "ds_read_b64_tr_b16 v[84:85], v121 offset:8192\n"
        "ds_read_b64_tr_b16 v[86:87], v121 offset:8704\n"
        "v_mfma_f32_16x16x32_bf16 v[28:31], v[88:91], v[60:63], 0\n"
        "ds_read_b64_tr_b16 v[88:89], v122 offset:8192\n"
        "ds_read_b64_tr_b16 v[90:91], v122 offset:8704\n"
        "v_mfma_f32_16x16x32_bf16 v[32:35], v[92:95], v[60:63], 0\n"
        "ds_read_b64_tr_b16 v[92:93], v123 offset:8192\n"
        "ds_read_b64_tr_b16 v[94:95], v123 offset:8704\n"
        "v_mfma_f32_16x16x32_bf16 v[36:39], v[96:99], v[60:63], 0\n"
        "ds_read_b64_tr_b16 v[96:97], v124 offset:2048\n"
        "ds_read_b64_tr_b16 v[98:99], v125 offset:2048\n"
        "s_waitcnt vmcnt(3) lgkmcnt(10)\n"
        "v_mfma_f32_16x16x32_bf16 v[20:23], v[100:103], v[64:67], v[20:23]\n"
        "ds_read_b64_tr_b16 v[100:101], v120 offset:12288\n"
        "ds_read_b64_tr_b16 v[102:103], v120 offset:12800\n"
        "v_mfma_f32_16x16x32_bf16 v[24:27], v[104:107], v[64:67], v[24:27]\n"
        "ds_read_b64_tr_b16 v[104:105], v121 offset:12288\n"
        "ds_read_b64_tr_b16 v[106:107], v121 offset:12800\n"
        "v_mfma_f32_16x16x32_bf16 v[28:31], v[108:111], v[64:67], v[28:31]\n"
        "ds_read_b64_tr_b16 v[108:109], v122 offset:12288\n"
        "ds_read_b64_tr_b16 v[110:111], v122 offset:12800\n"
        "v_mfma_f32_16x16x32_bf16 v[32:35], v[112:115], v[64:67], v[32:35]\n"
        "ds_read_b64_tr_b16 v[112:113], v123 offset:12288\n"
        "ds_read_b64_tr_b16 v[114:115], v123 offset:12800\n"
        "v_mfma_f32_16x16x32_bf16 v[36:39], v[116:119], v[64:67], v[36:39]\n"
        "ds_read_b64_tr_b16 v[116:117], v124 offset:3072\n"
        "ds_read_b64_tr_b16 v[118:119], v125 offset:3072\n"
        "s_waitcnt vmcnt(2) lgkmcnt(10)\n"
        "v_mfma_f32_16x16x32_bf16 v[20:23], v[80:83], v[68:71], v[20:23]\n"
        "ds_read_b64_tr_b16 v[80:81], v120 offset:16384\n"
        "ds_read_b64_tr_b16 v[82:83], v120 offset:16896\n"
        "v_mfma_f32_16x16x32_bf16 v[24:27], v[84:87], v[68:71], v[24:27]\n"
        "ds_read_b64_tr_b16 v[84:85], v121 offset:16384\n"
        "ds_read_b64_tr_b16 v[86:87], v121 offset:16896\n"
        "v_mfma_f32_16x16x32_bf16 v[28:31], v[88:91], v[68:71], v[28:31]\n"
        "ds_read_b64_tr_b16 v[88:89], v122 offset:16384\n"
        "ds_read_b64_tr_b16 v[90:91], v122 offset:16896\n"
        "v_mfma_f32_16x16x32_bf16 v[32:35], v[92:95], v[68:71], v[32:35]\n"
        "ds_read_b64_tr_b16 v[92:93], v123 offset:16384\n"
        "ds_read_b64_tr_b16 v[94:95], v123 offset:16896\n"
        "v_mfma_f32_16x16x32_bf16 v[36:39], v[96:99], v[68:71], v[36:39]\n"
        "ds_read_b64_tr_b16 v[96:97], v124 offset:4096\n"
        "ds_read_b64_tr_b16 v[98:99], v125 offset:4096\n"
        "s_waitcnt vmcnt(1) lgkmcnt(10)\n"
        "v_mfma_f32_16x16x32_bf16 v[20:23], v[100:103], v[72:75], v[20:23]\n"
        "v_mfma_f32_16x16x32_bf16 v[24:27], v[104:107], v[72:75], v[24:27]\n"
        "v_mfma_f32_16x16x32_bf16 v[28:31], v[108:111], v[72:75], v[28:31]\n"
        "v_mfma_f32_16x16x32_bf16 v[32:35], v[112:115], v[72:75], v[32:35]\n"
        "v_mfma_f32_16x16x32_bf16 v[36:39], v[116:119], v[72:75], v[36:39]\n"
        "s_waitcnt vmcnt(0) lgkmcnt(0)\n"
        "v_mfma_f32_16x16x32_bf16 v[20:23], v[80:83], v[76:79], v[20:23]\n"
        "v_mfma_f32_16x16x32_bf16 v[24:27], v[84:87], v[76:79], v[24:27]\n"
        "v_mfma_f32_16x16x32_bf16 v[28:31], v[88:91], v[76:79], v[28:31]\n"
        "v_mfma_f32_16x16x32_bf16 v[32:35], v[92:95], v[76:79], v[32:35]\n"
        "v_mfma_f32_16x16x32_bf16 v[36:39], v[96:99], v[76:79], v[36:39]\n"
        "s_nop 7\n"
      : "={v[0:15]}"(acc), "={v[16:19]}"(acc4), "={v[20:35]}"(t), "={v[36:39]}"(t4), "+{v[120:123]}"(an), "+{v124}"(b0), "+{v125}"(b1)
      : "{v126}"(oo), "{v127}"(L_OMASK), "{s[40:43]}"(srd), "{s44}"(ch * 5120), "{s45}"(ch * 5120 + 4096), "{s46}"(5120 - ch * 5120), "{s47}"(9216 - ch * 5120),
        "{s48}"(20480 - ch * 40960), "{s49}"((240u >> (4 * ch)) << (kh * 8)), "{s50}"(((pm & 255) + 1) >> 8)
      : "v40","v41","v42","v43","v44","v45","v46","v47","v48","v49","v50","v51","v52","v53","v54","v55","v56","v57","v58","v59","v60","v61","v62","v63","v64","v65","v66","v67","v68","v69","v70","v71","v72","v73","v74","v75","v76","v77","v78","v79","v80","v81","v82","v83","v84","v85","v86","v87","v88","v89","v90","v91","v92","v93","v94","v95","v96","v97","v98","v99","v100","v101","v102","v103","v104","v105","v106","v107","v108","v109","v110","v111","v112","v113","v114","v115","v116","v117","v118","v119", "s51", "s52", "scc", "memory");
    acc += t; acc4 += t4;
#pragma unroll
    for (int n = 0; n < 5; ++n) lds_st<f32x4>(L_FX + kh * 21504 + (m * 16 + l15) * 336 + n * 64 + g * 16, n < 4 ? f32x4{acc[4 * n & 15], acc[(4 * n + 1) & 15], acc[(4 * n + 2) & 15], acc[(4 * n + 3) & 15]} : acc4);
    BAR();
  }
}

extern "C" void kernel_launch(void* const* d_in, const int* in_sizes, int n_in, void* d_out, int out_size, void* d_ws, size_t ws_size, hipStream_t stream) {
  fused_kernel<<<256, 768, 0, stream>>>((const char*)d_in[0], (const char*)d_in[1], (const char*)d_in[2], (const char*)d_in[4], (const char*)d_in[5],
                                        (const char*)d_in[6], (const char*)d_in[7], (char*)d_out, (char*)d_ws);
}
```
